```python
import torch

def _softmax(x, axis=-1):
    return torch.softmax(x, dim=axis)
def _gelu(x):
    return torch.nn.functional.gelu(x, approximate="tanh")
def _silu(x):
    return torch.nn.functional.silu(x)
def _sigmoid(x):
    return torch.sigmoid(x)
def _layernorm(x, axis=-1, eps=1e-5):
    m = x.mean(dim=axis, keepdim=True)
    v = x.var(dim=axis, keepdim=True, unbiased=False)
    return (x - m) / torch.sqrt(v + eps)
def _scatter_add(tgt, idx, src, axis=0):
    return tgt.clone().index_add_(axis, idx, src)
def _segment_sum(src, seg, n):
    out = torch.zeros((n,) + src.shape[1:], dtype=src.dtype)
    return out.index_add_(0, seg, src)
def _pad(x, pw):
    flat = tuple(v for pair in reversed(pw) for v in pair)
    return torch.nn.functional.pad(x, flat)

kernel_name = 'parallel_sum__ssm_conv_h3__geglu_ffn__prompt'

def setup_inputs():

    _w = (lambda a: (lambda t: t.to(torch.bfloat16) if t.is_floating_point() else t)(torch.as_tensor(a)))
    def _A_setup():
        B, S, D = 1, 2048, 512
        ks = 4
        return {
            "q": _w((lambda *s: torch.randn(*s, dtype=torch.bfloat16) * 0.05)(B, S, D)),
            "k": _w((lambda *s: torch.randn(*s, dtype=torch.bfloat16) * 0.8)(B, S, D)),
            "v": _w((lambda *s: torch.randn(*s, dtype=torch.bfloat16) * 0.8)(B, S, D)),
            "shift_k": _w((lambda *s: torch.randn(*s, dtype=torch.bfloat16) * 0.8)(D, ks) * 0.5),
            "ssm_k": _w((lambda *s: torch.randn(*s, dtype=torch.bfloat16) * 0.8)(D, S) * (1.0 / S ** 0.5)),
            "ssm_D": _w((lambda *s: torch.randn(*s, dtype=torch.bfloat16) * 0.8)(D)),
        }
    _A_in = _A_setup()

    def _B_setup():
        B, S, D, F = 1, 2048, 512, 1792
        return {
            "x": _w((lambda *s: torch.randn(*s, dtype=torch.bfloat16) * 0.05)(B, S, D)),
            "w_gate": _w((lambda *s: torch.randn(*s, dtype=torch.bfloat16) * 0.05)(D, F)),
            "w_up": _w((lambda *s: torch.randn(*s, dtype=torch.bfloat16) * 0.05)(D, F)),
            "w_down": _w((lambda *s: torch.randn(*s, dtype=torch.bfloat16) * 0.05)(F, D)),
        }
    _B_in = _B_setup()

    return {
        "A_q": _A_in["q"],
        "A_k": _A_in["k"],
        "A_v": _A_in["v"],
        "A_shift_k": _A_in["shift_k"],
        "A_ssm_k": _A_in["ssm_k"],
        "A_ssm_D": _A_in["ssm_D"],
        "B_x": _B_in["x"],
        "B_w_gate": _B_in["w_gate"],
        "B_w_up": _B_in["w_up"],
        "B_w_down": _B_in["w_down"],
    }

def reference(A_q, A_k, A_v, A_shift_k, A_ssm_k, A_ssm_D, B_x, B_w_gate, B_w_up, B_w_down):
    def _A_ref(q, k, v, shift_k, ssm_k, ssm_D):
        B, S, D = q.shape
        ks = shift_k.shape[-1]
        k_pad = _pad(k, ((0, 0), (ks - 1, 0), (0, 0)))
        k_shift = torch.zeros_like(k)
        for t in range(ks):
            k_shift = k_shift + k_pad[:, t:t + S, :] * shift_k[:, ks - 1 - t][None, None, :]
        kv = k_shift * v
        L = 2 * S
        kv_pad = _pad(kv, ((0, 0), (0, S), (0, 0)))
        kf_pad = _pad(ssm_k, ((0, 0), (0, S)))
        kv_f = (lambda x, axes=(-2,-1): torch.fft.rfft2(x.float(), dim=axes))(kv_pad[:, None, :, :], axes=(1, 2))
        kf_f = (lambda x, axes=(-2,-1): torch.fft.rfft2(x.float(), dim=axes))(kf_pad[:, None, :], axes=(1, 2))
        kf_f = (lambda x, *d: x.permute(*d))(kf_f, 1, 2, 0)
        y_f = kv_f * kf_f[None, :, :, :]
        y = (lambda x, s, axes=(-2,-1): torch.fft.irfft2(x, s=s, dim=axes).to(torch.bfloat16))(y_f, (1, L), axes=(1, 2))[:, 0, :S, :]
        y = y + kv * ssm_D[None, None, :]
        return q * y

    def _B_ref(x, w_gate, w_up, w_down):
        return (_gelu(x @ w_gate) * (x @ w_up)) @ w_down

    x = A_q
    xn = _layernorm(x, axis=-1)
    return x + _A_ref(xn, A_k, A_v, A_shift_k, A_ssm_k, A_ssm_D) + _B_ref(xn, B_w_gate, B_w_up, B_w_down)
```

```cpp
#include <hip/hip_runtime.h>
#include <hip/hip_bf16.h>
typedef unsigned short u16;
constexpr int S=2048, D=512, F=1792;
__device__ __forceinline__ float bf2f(u16 v){ return __uint_as_float(((unsigned)v)<<16); }
__device__ __forceinline__ u16 f2bf(float f){ __hip_bfloat16 b = __float2bfloat16(f); return *reinterpret_cast<u16*>(&b); }
__device__ __forceinline__ float rb(float f){ return bf2f(f2bf(f)); }

__global__ void k_ln(const u16* x, u16* xn){
  int row = blockIdx.x; int t = threadIdx.x;
  float v[8]; float s=0;
  for(int j=0;j<8;j++){ v[j]=bf2f(x[row*D+t*8+j]); s+=v[j]; }
  for(int o=32;o>0;o>>=1) s+=__shfl_xor(s,o);
  float m=s/D; float q=0;
  for(int j=0;j<8;j++){ float d=v[j]-m; q+=d*d; }
  for(int o=32;o>0;o>>=1) q+=__shfl_xor(q,o);
  float var=rb(q/D); float mb=rb(m);
  float sd=rb(sqrtf(rb(var+1e-5f)));
  for(int j=0;j<8;j++) xn[row*D+t*8+j]=f2bf(rb(v[j]-mb)/sd);
}
__global__ void k_kv(const u16* k,const u16* v,const u16* sk,u16* kv){
  int i=blockIdx.x*256+threadIdx.x; int t=i/D,c=i%D;
  float acc=0;
  for(int j=0;j<4;j++){ int tt=t+j-3; float kk= tt>=0? bf2f(k[tt*D+c]):0.f; acc=rb(acc+rb(kk*bf2f(sk[c*4+3-j]))); }
  kv[i]=f2bf(acc*bf2f(v[i]));
}
__global__ void k_conv(const u16* kv,const u16* h,const u16* Dv,u16* y){
  __shared__ float su[S], sh[S];
  int c=blockIdx.x;
  for(int t=threadIdx.x;t<S;t+=256){ su[t]=bf2f(kv[t*D+c]); sh[t]=bf2f(h[c*S+t]); }
  __syncthreads();
  float dd=bf2f(Dv[c]);
  for(int t=threadIdx.x;t<S;t+=256){ float a=0; for(int s=0;s<=t;s++) a+=sh[t-s]*su[s]; y[t*D+c]=f2bf(rb(a)+rb(su[t]*dd)); }
}
__global__ void k_h(const u16* xn,const u16* wg,const u16* wu,u16* H){
  int row=blockIdx.y; int f=blockIdx.x*256+threadIdx.x;
  __shared__ float sx[D];
  for(int i=threadIdx.x;i<D;i+=256) sx[i]=bf2f(xn[row*D+i]);
  __syncthreads();
  float g=0,u=0;
  for(int k=0;k<D;k++){ g+=sx[k]*bf2f(wg[k*F+f]); u+=sx[k]*bf2f(wu[k*F+f]); }
  g=rb(g); u=rb(u);
  float ge=rb(0.5f*g*(1.f+tanhf(0.7978845608028654f*(g+0.044715f*g*g*g))));
  H[row*F+f]=f2bf(ge*u);
}
__global__ void k_out(const u16* x,const u16* xn,const u16* y,const u16* H,const u16* wd,u16* out){
  int row=blockIdx.y; int c=blockIdx.x*256+threadIdx.x;
  __shared__ float sh[F];
  for(int i=threadIdx.x;i<F;i+=256) sh[i]=bf2f(H[row*F+i]);
  __syncthreads();
  float a=0;
  for(int k=0;k<F;k++) a+=sh[k]*bf2f(wd[k*D+c]);
  float A=rb(bf2f(xn[row*D+c])*bf2f(y[row*D+c]));
  out[row*D+c]=f2bf(rb(bf2f(x[row*D+c])+A)+rb(a));
}
extern "C" void kernel_launch(void* const* d_in, const int* in_sizes, int n_in,
                              void* d_out, int out_size, void* d_ws, size_t ws_size,
                              hipStream_t stream) {
  const u16* q=(const u16*)d_in[0]; const u16* k=(const u16*)d_in[1]; const u16* v=(const u16*)d_in[2];
  const u16* sk=(const u16*)d_in[3]; const u16* h=(const u16*)d_in[4]; const u16* Dv=(const u16*)d_in[5];
  const u16* wg=(const u16*)d_in[7]; const u16* wu=(const u16*)d_in[8]; const u16* wd=(const u16*)d_in[9];
  u16* ws=(u16*)d_ws; u16* xn=ws; u16* kv=ws+S*D; u16* y=ws+2*S*D; u16* H=ws+3*S*D;
  k_ln<<<S,64,0,stream>>>(q,xn);
  k_kv<<<S*D/256,256,0,stream>>>(k,v,sk,kv);
  k_conv<<<D,256,0,stream>>>(kv,h,Dv,y);
  k_h<<<dim3(F/256,S),256,0,stream>>>(xn,wg,wu,H);
  k_out<<<dim3(D/256,S),256,0,stream>>>(q,xn,y,H,wd,(u16*)d_out);
}
```

```cpp
#define PROBE_BYTES 0
#define PROBE_SPAN 0
#include <hip/hip_runtime.h>
#include <hip/hip_bf16.h>
typedef unsigned short u16;
constexpr int S=2048, D=512, F=1792;
__device__ __forceinline__ float bf2f(u16 v){ return __uint_as_float(((unsigned)v)<<16); }
__device__ __forceinline__ u16 f2bf(float f){ __hip_bfloat16 b = __float2bfloat16(f); return *reinterpret_cast<u16*>(&b); }
__device__ __forceinline__ float rb(float f){ return bf2f(f2bf(f)); }

__global__ void k_ln(const u16* x, u16* xn){
  int row = blockIdx.x; int t = threadIdx.x;
  float v[8]; float s=0;
  for(int j=0;j<8;j++){ v[j]=bf2f(x[row*D+t*8+j]); s+=v[j]; }
  for(int o=32;o>0;o>>=1) s+=__shfl_xor(s,o);
  float m=s/D; float q=0;
  for(int j=0;j<8;j++){ float d=v[j]-m; q+=d*d; }
  for(int o=32;o>0;o>>=1) q+=__shfl_xor(q,o);
  float var=rb(q/D); float mb=rb(m);
  float sd=rb(sqrtf(rb(var+1e-5f)));
  for(int j=0;j<8;j++) xn[row*D+t*8+j]=f2bf(rb(v[j]-mb)/sd);
}
__global__ void k_kv(const u16* k,const u16* v,const u16* sk,u16* kv){
  int i=blockIdx.x*256+threadIdx.x; int t=i/D,c=i%D;
  float acc=0;
  for(int j=0;j<4;j++){ int tt=t+j-3; float kk= tt>=0? bf2f(k[tt*D+c]):0.f; acc=rb(acc+rb(kk*bf2f(sk[c*4+3-j]))); }
  kv[i]=f2bf(acc*bf2f(v[i]));
}
__global__ void k_conv(const u16* kv,const u16* h,const u16* Dv,u16* y){
  __shared__ float su[S], sh[S];
  int c=blockIdx.x;
  for(int t=threadIdx.x;t<S;t+=256){ su[t]=bf2f(kv[t*D+c]); sh[t]=bf2f(h[c*S+t]); }
  __syncthreads();
  float dd=bf2f(Dv[c]);
  for(int t=threadIdx.x;t<S;t+=256){ float a=0; for(int s=0;s<=t;s++) a+=sh[t-s]*su[s]; y[t*D+c]=f2bf(rb(a)+rb(su[t]*dd)); }
}
__global__ void k_h(const u16* xn,const u16* wg,const u16* wu,u16* H){
  int row=blockIdx.y; int f=blockIdx.x*256+threadIdx.x;
  __shared__ float sx[D];
  for(int i=threadIdx.x;i<D;i+=256) sx[i]=bf2f(xn[row*D+i]);
  __syncthreads();
  float g=0,u=0;
  for(int k=0;k<D;k++){ g+=sx[k]*bf2f(wg[k*F+f]); u+=sx[k]*bf2f(wu[k*F+f]); }
  g=rb(g); u=rb(u);
  float ge=rb(0.5f*g*(1.f+tanhf(0.7978845608028654f*(g+0.044715f*g*g*g))));
  H[row*F+f]=f2bf(ge*u);
}
__global__ void k_out(const u16* x,const u16* xn,const u16* y,const u16* H,const u16* wd,u16* out){
  int row=blockIdx.y; int c=blockIdx.x*256+threadIdx.x;
  __shared__ float sh[F];
  for(int i=threadIdx.x;i<F;i+=256) sh[i]=bf2f(H[row*F+i]);
  __syncthreads();
  float a=0;
  for(int k=0;k<F;k++) a+=sh[k]*bf2f(wd[k*D+c]);
  float A=rb(bf2f(xn[row*D+c])*bf2f(y[row*D+c]));
  out[row*D+c]=f2bf(rb(bf2f(x[row*D+c])+A)+rb(a));
}

typedef unsigned long long u64;
#define LAS __attribute__((address_space(3)))
#define GAS __attribute__((address_space(1)))
struct P{ const char* p[8]; };
__global__ __launch_bounds__(256) void k_probe(const char* q,const char* k,const char* v,const char* h,const char* wg,const char* wu,const char* wd, u64* rec){
  __shared__ char smem[61440];
  u64 r0=__builtin_amdgcn_s_memrealtime();
  int b=blockIdx.x; int lane=threadIdx.x&63; int w=__builtin_amdgcn_readfirstlane(threadIdx.x>>6);
  const char* s2[4]={q,k,v,h}; const char* s3[3]={wg,wu,wd};
  asm volatile("s_waitcnt vmcnt(0)":::"memory");
  __syncthreads();
  u64 r1=__builtin_amdgcn_s_memrealtime();
  if(threadIdx.x==0){ rec[b*2]=r0; rec[b*2+1]=r1 + (smem[1234]==77 && smem[4321]==99 && smem[999]==3 && smem[555]==4? 0:0); }
}
__global__ void k_amp(const u64* rec){
  int l=threadIdx.x; u64 s=0, mn=~0ull, mx=0;
  for(int i=0;i<4;i++){ u64 a=rec[(l*4+i)*2], b=rec[(l*4+i)*2+1]; s+=b-a; mn=a<mn?a:mn; mx=b>mx?b:mx; }
  for(int o=32;o>0;o>>=1){ s+=__shfl_xor(s,o); u64 m2=__shfl_xor(mn,o); mn=m2<mn?m2:mn; u64 m3=__shfl_xor(mx,o); mx=m3>mx?m3:mx; }
  u64 d = PROBE_SPAN ? (mx-mn)*100 : s*100/256;
  if(d>100000) d=100000;
  u64 t0=__builtin_amdgcn_s_memrealtime();
  while(__builtin_amdgcn_s_memrealtime()-t0<d) __builtin_amdgcn_s_sleep(2);
}
extern "C" void kernel_launch(void* const* d_in, const int* in_sizes, int n_in,
                              void* d_out, int out_size, void* d_ws, size_t ws_size,
                              hipStream_t stream) {
  const u16* q=(const u16*)d_in[0]; const u16* k=(const u16*)d_in[1]; const u16* v=(const u16*)d_in[2];
  const u16* sk=(const u16*)d_in[3]; const u16* h=(const u16*)d_in[4]; const u16* Dv=(const u16*)d_in[5];
  const u16* wg=(const u16*)d_in[7]; const u16* wu=(const u16*)d_in[8]; const u16* wd=(const u16*)d_in[9];
  u16* ws=(u16*)d_ws; u16* xn=ws; u16* kv=ws+S*D; u16* y=ws+2*S*D; u16* H=ws+3*S*D;
  u64* rec=(u64*)(ws+16*S*D);
  k_probe<<<256,256,0,stream>>>((const char*)q,(const char*)k,(const char*)v,(const char*)h,(const char*)wg,(const char*)wu,(const char*)wd,rec);
  k_amp<<<1,64,0,stream>>>(rec);
  k_ln<<<S,64,0,stream>>>(q,xn);
  k_kv<<<S*D/256,256,0,stream>>>(k,v,sk,kv);
  k_conv<<<D,256,0,stream>>>(kv,h,Dv,y);
  k_h<<<dim3(F/256,S),256,0,stream>>>(xn,wg,wu,H);
  k_out<<<dim3(D/256,S),256,0,stream>>>(q,xn,y,H,wd,(u16*)d_out);
}
```

```cpp
#include <hip/hip_runtime.h>
__global__ __launch_bounds__(256) void h3_geglu_kernel(const void* q, const void* k, const void* v, const void* sk, const void* h, const void* Dv,
                                                       const void* wg, const void* wu, const void* wd, void* out, void* ws) {
  unsigned tid = threadIdx.x;
  asm volatile(
".L_start%=:\n"
"s_mov_b64 s[4:5], %1\n"
"s_mov_b32 s6, %2\n"
"s_load_dwordx16 s[64:79], s[4:5], 0\n"
"s_load_dwordx4 s[80:83], s[4:5], 64\n"
"s_load_dwordx2 s[84:85], s[4:5], 80\n"
"v_lshrrev_b32 v1, 6, v0\n"
"v_and_b32 v0, 63, v0\n"
"s_and_b32 s22, s6, 7\n"
"s_lshr_b32 s23, s6, 3\n"
"v_readfirstlane_b32 s7, v1\n"
"s_and_b32 s20, s22, 3\n"
"s_lshl_b32 s20, s20, 2\n"
"s_and_b32 s24, s23, 3\n"
"s_add_u32 s20, s20, s24\n"
"s_lshr_b32 s21, s22, 2\n"
"s_mul_i32 s21, s21, 7\n"
"s_lshr_b32 s24, s23, 2\n"
"s_add_u32 s21, s21, s24\n"
"v_lshrrev_b32 v1, 4, v0\n"
"v_and_b32 v2, 15, v0\n"
"s_and_b32 s24, s7, 1\n"
"s_lshl_b32 s25, s24, 4\n"
"v_add_u32 v3, s25, v1\n"
"v_mul_u32_u24 v3, 0xe00, v3\n"
"s_lshl_b32 s25, s21, 8\n"
"v_add_u32 v3, s25, v3\n"
"v_lshlrev_b32 v4, 1, v1\n"
"v_xor_b32 v4, v4, v2\n"
"v_lshl_add_u32 v8, v4, 4, v3\n"
"v_add_u32 v9, 0x3400, v8\n"
"v_xor_b32 v4, 8, v4\n"
"v_lshl_add_u32 v4, v4, 4, v3\n"
"v_add_u32 v10, 0x6800, v4\n"
"v_add_u32 v11, 0x9c00, v4\n"
"s_lshl_b32 s25, s20, 7\n"
"s_lshl_b32 s26, s7, 5\n"
"s_add_u32 s25, s25, s26\n"
"v_lshrrev_b32 v3, 3, v0\n"
"v_add_u32 v3, s25, v3\n"
"v_lshlrev_b32 v3, 10, v3\n"
"v_and_b32 v4, 7, v0\n"
"v_xor_b32 v4, v4, v1\n"
"v_lshl_add_u32 v12, v4, 4, v3\n"
"v_add_u32 v14, 0x3800, v12\n"
"v_xor_b32 v4, 4, v4\n"
"v_lshl_add_u32 v4, v4, 4, v3\n"
"v_add_u32 v13, 0x1c00, v4\n"
"v_add_u32 v15, 0x5400, v4\n"
"s_lshl_b32 s26, s7, 12\n"
"s_add_u32 s27, s26, 0x14000\n"
"s_waitcnt lgkmcnt(0)\n"
"s_mov_b32 s8, s64\n"
"s_and_b32 s9, s65, 0xffff\n"
"s_mov_b32 s10, 0x200000\n"
"s_mov_b32 s11, 0x20000\n"
"s_cmp_ge_u32 s23, 28\n"
"s_cbranch_scc1 .L_prep%=\n"
"s_cmp_ge_u32 s7, 2\n"
"s_cselect_b32 s12, s78, s76\n"
"s_cselect_b32 s13, s79, s77\n"
"s_and_b32 s13, s13, 0xffff\n"
"s_mov_b32 s14, 0x1c0000\n"
"s_mov_b32 s15, 0x20000\n"
"s_add_u32 m0, s27, 0x0\n"
"s_movk_i32 s25, 0x0\n"
"buffer_load_dwordx4 v12, s[8:11], s25 offen offset:0 lds\n"
"buffer_load_dwordx4 v13, s[8:11], s25 offen offset:1024 lds\n"
"buffer_load_dwordx4 v14, s[8:11], s25 offen offset:2048 lds\n"
"v_bfe_u32 v4, v0, 1, 3\n"
"v_xor_b32 v4, v4, v1\n"
"s_lshr_b32 s28, s7, 1\n"
"s_lshl_b32 s28, s28, 13\n"
"s_add_u32 s28, s28, 0x14000\n"
"v_lshl_add_u32 v3, v2, 7, s28\n"
"buffer_load_dwordx4 v15, s[8:11], s25 offen offset:3072 lds\n"
"v_lshl_add_u32 v16, v4, 4, v3\n"
"v_xor_b32 v17, 64, v16\n"
"v_bfe_u32 v4, v0, 2, 2\n"
"v_lshl_add_u32 v5, v1, 3, v4\n"
"v_lshlrev_b32 v5, 8, v5\n"
"v_and_b32 v6, 3, v0\n"
"s_add_u32 m0, s26, 0x0\n"
"s_mov_b32 s24, 0x0\n"
"buffer_load_dwordx4 v8, s[12:15], s24 offen offset:0 lds\n"
"v_lshl_add_u32 v5, v6, 3, v5\n"
"v_bfe_u32 v6, v0, 4, 1\n"
"v_lshl_or_b32 v4, v6, 2, v4\n"
"s_and_b32 s28, s7, 1\n"
"s_lshl_b32 s28, s28, 2\n"
"v_xor_b32 v4, s28, v4\n"
"buffer_load_dwordx4 v9, s[12:15], s24 offen offset:1024 lds\n"
"v_lshl_add_u32 v18, v4, 5, v5\n"
"v_xor_b32 v19, 32, v18\n"
"v_xor_b32 v20, 64, v18\n"
"v_xor_b32 v21, 0x60, v18\n"
"v_add_u32 v22, 0x10000, v18\n"
"v_add_u32 v23, 0x10000, v19\n"
"buffer_load_dwordx4 v10, s[12:15], s24 offen offset:2048 lds\n"
"v_add_u32 v24, 0x10000, v20\n"
"v_add_u32 v25, 0x10000, v21\n"
"s_mov_b32 s36, s72\n"
"s_and_b32 s37, s73, 0xffff\n"
"s_mov_b32 s38, 0x200000\n"
"s_mov_b32 s39, 0x20000\n"
"buffer_load_dwordx4 v11, s[12:15], s24 offen offset:3072 lds\n"
"s_mov_b32 s16, s84\n"
"s_and_b32 s17, s85, 0xffff\n"
"s_mov_b32 s18, 0xd00000\n"
"s_mov_b32 s19, 0x20000\n"
"v_mov_b32 v30, 0\n"
"buffer_load_dwordx2 v[32:33], v30, s[16:19], 0 offen sc1\n"
"s_cmp_lg_u32 s7, 0\n"
"s_cbranch_scc1 .L_nopf1_%=\n"
"s_getpc_b64 s[28:29]\n"
".L_pc1_%=:\n"
"s_sub_u32 s28, s28, .L_pc1_%=-.L_start%=\n"
"s_subb_u32 s29, s29, 0\n"
"s_and_b32 s30, s23, 7\n"
"s_lshl_b32 s30, s30, 13\n"
"v_lshl_add_u32 v31, v0, 7, s30\n"
"v_min_u32 v31, .L_end%=-.L_start%=-4, v31\n"
"global_load_dword v34, v31, s[28:29]\n"
".L_nopf1_%=:\n"
"s_add_u32 m0, s26, 0x4000\n"
"s_mov_b32 s24, 0x1c000\n"
"buffer_load_dwordx4 v8, s[12:15], s24 offen offset:0 lds\n"
"s_mov_b32 s40, s80\n"
"s_and_b32 s41, s81, 0xffff\n"
"s_mov_b32 s42, 0x1c0000\n"
"s_mov_b32 s43, 0x20000\n"
"s_mov_b32 s44, s82\n"
"s_and_b32 s45, s83, 0xffff\n"
"buffer_load_dwordx4 v9, s[12:15], s24 offen offset:1024 lds\n"
"s_mov_b32 s46, 0x200000\n"
"s_mov_b32 s47, 0x20000\n"
"s_mov_b32 s88, s84\n"
"s_and_b32 s89, s85, 0xffff\n"
"s_mov_b32 s90, 0xd00000\n"
"s_mov_b32 s91, 0x20000\n"
"buffer_load_dwordx4 v10, s[12:15], s24 offen offset:2048 lds\n"
"s_mov_b32 s92, s66\n"
"s_and_b32 s93, s67, 0xffff\n"
"s_mov_b32 s94, 0x200000\n"
"s_mov_b32 s95, 0x20000\n"
"s_mov_b32 s96, s68\n"
"s_and_b32 s97, s69, 0xffff\n"
"buffer_load_dwordx4 v11, s[12:15], s24 offen offset:3072 lds\n"
"s_mov_b32 s98, 0x200000\n"
"s_mov_b32 s99, 0x20000\n"
"s_mov_b64 s[50:51], s[74:75]\n"
"s_mov_b64 s[56:57], s[70:71]\n"
"s_and_b32 s48, s22, 3\n"
"s_lshl_b32 s48, s48, 3\n"
"s_add_u32 m0, s27, 0x4000\n"
"s_movk_i32 s25, 0x80\n"
"buffer_load_dwordx4 v12, s[8:11], s25 offen offset:0 lds\n"
"s_lshr_b32 s28, s22, 2\n"
"s_lshl_b32 s28, s28, 2\n"
"s_add_u32 s48, s48, s28\n"
"s_and_b32 s28, s23, 3\n"
"s_add_u32 s48, s48, s28\n"
"s_lshr_b32 s49, s23, 2\n"
"buffer_load_dwordx4 v13, s[8:11], s25 offen offset:1024 lds\n"
"s_and_b32 s31, s48, 3\n"
"s_lshl_b32 s86, s31, 17\n"
"buffer_load_dwordx4 v14, s[8:11], s25 offen offset:2048 lds\n"
"buffer_load_dwordx4 v15, s[8:11], s25 offen offset:3072 lds\n"
"s_add_u32 m0, s26, 0x8000\n"
"s_mov_b32 s24, 0x38000\n"
"buffer_load_dwordx4 v8, s[12:15], s24 offen offset:0 lds\n"
"buffer_load_dwordx4 v9, s[12:15], s24 offen offset:1024 lds\n"
"buffer_load_dwordx4 v10, s[12:15], s24 offen offset:2048 lds\n"
"buffer_load_dwordx4 v11, s[12:15], s24 offen offset:3072 lds\n"
"s_add_u32 m0, s26, 0xc000\n"
"s_mov_b32 s24, 0x54000\n"
"buffer_load_dwordx4 v8, s[12:15], s24 offen offset:0 lds\n"
"buffer_load_dwordx4 v9, s[12:15], s24 offen offset:1024 lds\n"
"buffer_load_dwordx4 v10, s[12:15], s24 offen offset:2048 lds\n"
"buffer_load_dwordx4 v11, s[12:15], s24 offen offset:3072 lds\n"
"s_bfe_u32 s29, s6, 0x30003\n"
"s_lshl_b32 s28, s29, 9\n"
"v_lshl_add_u32 v53, v0, 3, s28\n"
"global_load_dwordx2 v[62:63], v53, s[56:57]\n"
"s_and_b32 s56, s6, 7\n"
"s_lshl_b32 s56, s56, 2\n"
"s_lshr_b32 s28, s6, 6\n"
"s_add_u32 s56, s56, s28\n"
"s_bfe_u32 s57, s6, 0x30003\n"
"s_lshl_b32 s28, s56, 16\n"
"s_lshl_b32 s29, s7, 14\n"
"s_add_u32 s28, s28, s29\n"
"s_lshl_b32 s29, s57, 7\n"
"s_add_u32 s28, s28, s29\n"
"v_lshrrev_b32 v51, 3, v0\n"
"v_lshlrev_b32 v51, 11, v51\n"
"v_and_b32 v53, 7, v0\n"
"v_lshl_add_u32 v51, v53, 4, v51\n"
"v_add_u32 v51, s28, v51\n"
"v_subrev_u32 v53, 0xc00, v51\n"
"s_waitcnt vmcnt(18)\n"
"s_barrier\n"
"ds_read_b64_tr_b16 a[16:17], v18 offset:0\n"
"buffer_load_dwordx4 v[64:67], v53, s[92:95], 0 offen\n"
"ds_read_b64_tr_b16 a[18:19], v18 offset:1024\n"
"ds_read_b128 a[0:3], v16 offset:0\n"
"v_subrev_u32 v53, 0x800, v51\n"
"buffer_load_dwordx4 v[68:71], v53, s[92:95], 0 offen\n"
"ds_read_b128 a[4:7], v16 offset:2048\n"
"ds_read_b128 a[8:11], v16 offset:4096\n"
"ds_read_b128 a[12:15], v16 offset:6144\n"
"v_subrev_u32 v53, 0x400, v51\n"
"buffer_load_dwordx4 v[72:75], v53, s[92:95], 0 offen\n"
"ds_read_b64_tr_b16 a[20:21], v19 offset:0\n"
"ds_read_b64_tr_b16 a[22:23], v19 offset:1024\n"
"ds_read_b64_tr_b16 a[24:25], v20 offset:0\n"
"buffer_load_dwordx4 v[76:79], v51, s[92:95], 0 offen offset:0\n"
"ds_read_b64_tr_b16 a[26:27], v20 offset:1024\n"
"ds_read_b64_tr_b16 a[28:29], v21 offset:0\n"
"ds_read_b64_tr_b16 a[30:31], v21 offset:1024\n"
"buffer_load_dwordx4 v[80:83], v51, s[92:95], 0 offen offset:1024\n"
"ds_read_b64_tr_b16 a[32:33], v18 offset:8192\n"
"ds_read_b64_tr_b16 a[34:35], v18 offset:9216\n"
"ds_read_b64_tr_b16 a[36:37], v19 offset:8192\n"
"buffer_load_dwordx4 v[84:87], v51, s[96:99], 0 offen offset:0\n"
"ds_read_b64_tr_b16 a[38:39], v19 offset:9216\n"
"ds_read_b64_tr_b16 a[40:41], v20 offset:8192\n"
"ds_read_b64_tr_b16 a[42:43], v20 offset:9216\n"
"buffer_load_dwordx4 v[88:91], v51, s[96:99], 0 offen offset:1024\n"
"ds_read_b64_tr_b16 a[44:45], v21 offset:8192\n"
"ds_read_b64_tr_b16 a[46:47], v21 offset:9216\n"
"s_waitcnt vmcnt(20) lgkmcnt(0)\n"
"s_barrier\n"
"v_mfma_f32_16x16x32_bf16 v[128:131], a[16:19], a[0:3], 0\n"
"ds_read_b64_tr_b16 a[64:65], v18 offset:16384\n"
"s_add_u32 m0, s27, 0x8000\n"
"s_movk_i32 s25, 0x100\n"
"v_mfma_f32_16x16x32_bf16 v[132:135], a[16:19], a[4:7], 0\n"
"buffer_load_dwordx4 v12, s[8:11], s25 offen offset:0 lds\n"
"s_lshl_b32 s52, s20, 1\n"
"v_mfma_f32_16x16x32_bf16 v[136:139], a[16:19], a[8:11], 0\n"
"ds_read_b64_tr_b16 a[66:67], v18 offset:17408\n"
"s_lshr_b32 s53, s7, 1\n"
"v_mfma_f32_16x16x32_bf16 v[140:143], a[16:19], a[12:15], 0\n"
"ds_read_b128 a[48:51], v17 offset:0\n"
"s_add_u32 s52, s52, s53\n"
"v_mfma_f32_16x16x32_bf16 v[144:147], a[20:23], a[0:3], 0\n"
"ds_read_b128 a[52:55], v17 offset:2048\n"
"s_lshl_b32 s52, s52, 6\n"
"v_mfma_f32_16x16x32_bf16 v[148:151], a[20:23], a[4:7], 0\n"
"buffer_load_dwordx4 v13, s[8:11], s25 offen offset:1024 lds\n"
"s_add_u32 s52, s52, 0x1000\n"
"v_mfma_f32_16x16x32_bf16 v[152:155], a[20:23], a[8:11], 0\n"
"ds_read_b128 a[56:59], v17 offset:4096\n"
"v_lshl_add_u32 v127, v0, 3, s52\n"
"v_mfma_f32_16x16x32_bf16 v[156:159], a[20:23], a[12:15], 0\n"
"ds_read_b128 a[60:63], v17 offset:6144\n"
"s_lshl_b32 s52, s21, 6\n"
"v_mfma_f32_16x16x32_bf16 v[160:163], a[24:27], a[0:3], 0\n"
"ds_read_b64_tr_b16 a[68:69], v19 offset:16384\n"
"s_and_b32 s53, s7, 1\n"
"v_mfma_f32_16x16x32_bf16 v[164:167], a[24:27], a[4:7], 0\n"
"buffer_load_dwordx4 v14, s[8:11], s25 offen offset:2048 lds\n"
"s_lshl_b32 s53, s53, 3\n"
"v_mfma_f32_16x16x32_bf16 v[168:171], a[24:27], a[8:11], 0\n"
"ds_read_b64_tr_b16 a[70:71], v19 offset:17408\n"
"s_add_u32 s52, s52, s53\n"
"v_mfma_f32_16x16x32_bf16 v[172:175], a[24:27], a[12:15], 0\n"
"ds_read_b64_tr_b16 a[72:73], v20 offset:16384\n"
"s_add_u32 s52, s52, 0x2000\n"
"v_mfma_f32_16x16x32_bf16 v[176:179], a[28:31], a[0:3], 0\n"
"ds_read_b64_tr_b16 a[74:75], v20 offset:17408\n"
"s_add_u32 s53, s52, 896\n"
"v_mfma_f32_16x16x32_bf16 v[180:183], a[28:31], a[4:7], 0\n"
"buffer_load_dwordx4 v15, s[8:11], s25 offen offset:3072 lds\n"
"v_mov_b32 v3, s52\n"
"v_mfma_f32_16x16x32_bf16 v[184:187], a[28:31], a[8:11], 0\n"
"ds_read_b64_tr_b16 a[76:77], v21 offset:16384\n"
"v_mov_b32 v4, s53\n"
"v_mfma_f32_16x16x32_bf16 v[188:191], a[28:31], a[12:15], 0\n"
"ds_read_b64_tr_b16 a[78:79], v21 offset:17408\n"
"s_add_u32 m0, s26, 0x10000\n"
"v_mfma_f32_16x16x32_bf16 v[192:195], a[32:35], a[0:3], 0\n"
"ds_read_b64_tr_b16 a[80:81], v18 offset:24576\n"
"s_mov_b32 s24, 0x70000\n"
"v_mfma_f32_16x16x32_bf16 v[196:199], a[32:35], a[4:7], 0\n"
"buffer_load_dwordx4 v8, s[12:15], s24 offen offset:0 lds\n"
"v_cmp_eq_u32 vcc, 4, v0\n"
"v_mfma_f32_16x16x32_bf16 v[200:203], a[32:35], a[8:11], 0\n"
"ds_read_b64_tr_b16 a[82:83], v18 offset:25600\n"
"v_cndmask_b32 v127, v127, v3, vcc\n"
"v_mfma_f32_16x16x32_bf16 v[204:207], a[32:35], a[12:15], 0\n"
"ds_read_b64_tr_b16 a[84:85], v19 offset:24576\n"
"v_cmp_lt_u32 vcc, 4, v0\n"
"v_mfma_f32_16x16x32_bf16 v[208:211], a[36:39], a[0:3], 0\n"
"ds_read_b64_tr_b16 a[86:87], v19 offset:25600\n"
"v_cndmask_b32 v127, v127, v4, vcc\n"
"v_mfma_f32_16x16x32_bf16 v[212:215], a[36:39], a[4:7], 0\n"
"buffer_load_dwordx4 v9, s[12:15], s24 offen offset:1024 lds\n"
"v_bfe_u32 v3, v0, 2, 2\n"
"v_mfma_f32_16x16x32_bf16 v[216:219], a[36:39], a[8:11], 0\n"
"ds_read_b64_tr_b16 a[88:89], v20 offset:24576\n"
"v_lshl_add_u32 v3, v1, 2, v3\n"
"v_mfma_f32_16x16x32_bf16 v[220:223], a[36:39], a[12:15], 0\n"
"ds_read_b64_tr_b16 a[90:91], v20 offset:25600\n"
"v_bfe_u32 v4, v3, 1, 2\n"
"v_mfma_f32_16x16x32_bf16 v[224:227], a[40:43], a[0:3], 0\n"
"ds_read_b64_tr_b16 a[92:93], v21 offset:24576\n"
"v_lshlrev_b32 v3, 7, v3\n"
"v_mfma_f32_16x16x32_bf16 v[228:231], a[40:43], a[4:7], 0\n"
"buffer_load_dwordx4 v10, s[12:15], s24 offen offset:2048 lds\n"
"v_lshl_add_u32 v3, v4, 5, v3\n"
"v_mfma_f32_16x16x32_bf16 v[232:235], a[40:43], a[8:11], 0\n"
"ds_read_b64_tr_b16 a[94:95], v21 offset:25600\n"
"v_and_b32 v4, 3, v0\n"
"v_mfma_f32_16x16x32_bf16 v[236:239], a[40:43], a[12:15], 0\n"
"v_lshl_add_u32 v29, v4, 3, v3\n"
"v_mfma_f32_16x16x32_bf16 v[240:243], a[44:47], a[0:3], 0\n"
"v_mfma_f32_16x16x32_bf16 v[244:247], a[44:47], a[4:7], 0\n"
"buffer_load_dwordx4 v11, s[12:15], s24 offen offset:3072 lds\n"
"v_mfma_f32_16x16x32_bf16 v[248:251], a[44:47], a[8:11], 0\n"
"v_mfma_f32_16x16x32_bf16 v[252:255], a[44:47], a[12:15], 0\n"
"s_waitcnt vmcnt(20) lgkmcnt(0)\n"
"s_barrier\n"
"v_mfma_f32_16x16x32_bf16 v[128:131], a[64:67], a[48:51], v[128:131]\n"
"ds_read_b64_tr_b16 a[16:17], v18 offset:32768\n"
"s_add_u32 m0, s26, 0x0\n"
"s_mov_b32 s24, 0x8c000\n"
"v_mfma_f32_16x16x32_bf16 v[132:135], a[64:67], a[52:55], v[132:135]\n"
"buffer_load_dwordx4 v8, s[12:15], s24 offen offset:0 lds\n"
"v_mfma_f32_16x16x32_bf16 v[136:139], a[64:67], a[56:59], v[136:139]\n"
"ds_read_b64_tr_b16 a[18:19], v18 offset:33792\n"
"v_mfma_f32_16x16x32_bf16 v[140:143], a[64:67], a[60:63], v[140:143]\n"
"ds_read_b128 a[0:3], v16 offset:16384\n"
"v_mfma_f32_16x16x32_bf16 v[144:147], a[68:71], a[48:51], v[144:147]\n"
"ds_read_b128 a[4:7], v16 offset:18432\n"
"v_mfma_f32_16x16x32_bf16 v[148:151], a[68:71], a[52:55], v[148:151]\n"
"buffer_load_dwordx4 v9, s[12:15], s24 offen offset:1024 lds\n"
"v_mfma_f32_16x16x32_bf16 v[152:155], a[68:71], a[56:59], v[152:155]\n"
"ds_read_b128 a[8:11], v16 offset:20480\n"
"v_mfma_f32_16x16x32_bf16 v[156:159], a[68:71], a[60:63], v[156:159]\n"
"ds_read_b128 a[12:15], v16 offset:22528\n"
"v_mfma_f32_16x16x32_bf16 v[160:163], a[72:75], a[48:51], v[160:163]\n"
"ds_read_b64_tr_b16 a[20:21], v19 offset:32768\n"
"v_mfma_f32_16x16x32_bf16 v[164:167], a[72:75], a[52:55], v[164:167]\n"
"buffer_load_dwordx4 v10, s[12:15], s24 offen offset:2048 lds\n"
"v_mfma_f32_16x16x32_bf16 v[168:171], a[72:75], a[56:59], v[168:171]\n"
"ds_read_b64_tr_b16 a[22:23], v19 offset:33792\n"
"s_waitcnt vmcnt(35)\n"
"v_mfma_f32_16x16x32_bf16 v[172:175], a[72:75], a[60:63], v[172:175]\n"
"ds_read_b64_tr_b16 a[24:25], v20 offset:32768\n"
"v_readfirstlane_b32 s58, v32\n"
"v_mfma_f32_16x16x32_bf16 v[176:179], a[76:79], a[48:51], v[176:179]\n"
"ds_read_b64_tr_b16 a[26:27], v20 offset:33792\n"
"v_readfirstlane_b32 s59, v33\n"
"v_mfma_f32_16x16x32_bf16 v[180:183], a[76:79], a[52:55], v[180:183]\n"
"buffer_load_dwordx4 v11, s[12:15], s24 offen offset:3072 lds\n"
"s_mul_i32 s34, s58, 0x9e3779b1\n"
"v_mfma_f32_16x16x32_bf16 v[184:187], a[76:79], a[56:59], v[184:187]\n"
"ds_read_b64_tr_b16 a[28:29], v21 offset:32768\n"
"s_add_u32 s34, s34, 0x7f4a7c15\n"
"v_mfma_f32_16x16x32_bf16 v[188:191], a[76:79], a[60:63], v[188:191]\n"
"ds_read_b64_tr_b16 a[30:31], v21 offset:33792\n"
"s_add_u32 m0, s27, 0xc000\n"
"v_mfma_f32_16x16x32_bf16 v[192:195], a[80:83], a[48:51], v[192:195]\n"
"ds_read_b64_tr_b16 a[32:33], v18 offset:40960\n"
"s_movk_i32 s25, 0x180\n"
"v_mfma_f32_16x16x32_bf16 v[196:199], a[80:83], a[52:55], v[196:199]\n"
"buffer_load_dwordx4 v12, s[8:11], s25 offen offset:0 lds\n"
"s_xor_b32 s59, s59, s34\n"
"v_mfma_f32_16x16x32_bf16 v[200:203], a[80:83], a[56:59], v[200:203]\n"
"ds_read_b64_tr_b16 a[34:35], v18 offset:41984\n"
"s_mul_i32 s35, s59, 0x85ebca77\n"
"v_mfma_f32_16x16x32_bf16 v[204:207], a[80:83], a[60:63], v[204:207]\n"
"ds_read_b64_tr_b16 a[36:37], v19 offset:40960\n"
"s_add_u32 s35, s35, 0x165667b1\n"
"v_mfma_f32_16x16x32_bf16 v[208:211], a[84:87], a[48:51], v[208:211]\n"
"ds_read_b64_tr_b16 a[38:39], v19 offset:41984\n"
"v_mfma_f32_16x16x32_bf16 v[212:215], a[84:87], a[52:55], v[212:215]\n"
"buffer_load_dwordx4 v13, s[8:11], s25 offen offset:1024 lds\n"
"v_mfma_f32_16x16x32_bf16 v[216:219], a[84:87], a[56:59], v[216:219]\n"
"ds_read_b64_tr_b16 a[40:41], v20 offset:40960\n"
"v_mfma_f32_16x16x32_bf16 v[220:223], a[84:87], a[60:63], v[220:223]\n"
"ds_read_b64_tr_b16 a[42:43], v20 offset:41984\n"
"v_mfma_f32_16x16x32_bf16 v[224:227], a[88:91], a[48:51], v[224:227]\n"
"ds_read_b64_tr_b16 a[44:45], v21 offset:40960\n"
"v_mfma_f32_16x16x32_bf16 v[228:231], a[88:91], a[52:55], v[228:231]\n"
"buffer_load_dwordx4 v14, s[8:11], s25 offen offset:2048 lds\n"
"v_mfma_f32_16x16x32_bf16 v[232:235], a[88:91], a[56:59], v[232:235]\n"
"ds_read_b64_tr_b16 a[46:47], v21 offset:41984\n"
"v_mfma_f32_16x16x32_bf16 v[236:239], a[88:91], a[60:63], v[236:239]\n"
"v_mfma_f32_16x16x32_bf16 v[240:243], a[92:95], a[48:51], v[240:243]\n"
"v_mfma_f32_16x16x32_bf16 v[244:247], a[92:95], a[52:55], v[244:247]\n"
"buffer_load_dwordx4 v15, s[8:11], s25 offen offset:3072 lds\n"
"v_mfma_f32_16x16x32_bf16 v[248:251], a[92:95], a[56:59], v[248:251]\n"
"v_mfma_f32_16x16x32_bf16 v[252:255], a[92:95], a[60:63], v[252:255]\n"
"s_waitcnt vmcnt(24) lgkmcnt(0)\n"
"s_barrier\n"
"v_mfma_f32_16x16x32_bf16 v[128:131], a[16:19], a[0:3], v[128:131]\n"
"ds_read_b64_tr_b16 a[64:65], v18 offset:49152\n"
"s_add_u32 m0, s26, 0x4000\n"
"s_mov_b32 s24, 0xa8000\n"
"v_mfma_f32_16x16x32_bf16 v[132:135], a[16:19], a[4:7], v[132:135]\n"
"buffer_load_dwordx4 v8, s[12:15], s24 offen offset:0 lds\n"
"s_lshl_b32 s28, s7, 9\n"
"v_mfma_f32_16x16x32_bf16 v[136:139], a[16:19], a[8:11], v[136:139]\n"
"ds_read_b64_tr_b16 a[66:67], v18 offset:50176\n"
"s_add_u32 s28, s28, 153600\n"
"v_mfma_f32_16x16x32_bf16 v[140:143], a[16:19], a[12:15], v[140:143]\n"
"ds_read_b128 a[48:51], v17 offset:16384\n"
"v_lshl_add_u32 v53, v0, 3, s28\n"
"v_mfma_f32_16x16x32_bf16 v[144:147], a[20:23], a[0:3], v[144:147]\n"
"ds_read_b128 a[52:55], v17 offset:18432\n"
"s_waitcnt vmcnt(24)\n"
"v_mfma_f32_16x16x32_bf16 v[148:151], a[20:23], a[4:7], v[148:151]\n"
"buffer_load_dwordx4 v9, s[12:15], s24 offen offset:1024 lds\n"
"ds_write_b64 v53, v[62:63]\n"
"v_mfma_f32_16x16x32_bf16 v[152:155], a[20:23], a[8:11], v[152:155]\n"
"ds_read_b128 a[56:59], v17 offset:20480\n"
"v_and_b32 v53, 7, v0\n"
"v_mfma_f32_16x16x32_bf16 v[156:159], a[20:23], a[12:15], v[156:159]\n"
"ds_read_b128 a[60:63], v17 offset:22528\n"
"v_lshl_add_u32 v53, v53, 6, s28\n"
"v_mfma_f32_16x16x32_bf16 v[160:163], a[24:27], a[0:3], v[160:163]\n"
"ds_read_b64_tr_b16 a[68:69], v19 offset:49152\n"
"s_mul_i32 s52, s7, 448\n"
"v_mfma_f32_16x16x32_bf16 v[164:167], a[24:27], a[4:7], v[164:167]\n"
"buffer_load_dwordx4 v10, s[12:15], s24 offen offset:2048 lds\n"
"v_lshrrev_b32 v3, 3, v0\n"
"v_mfma_f32_16x16x32_bf16 v[168:171], a[24:27], a[8:11], v[168:171]\n"
"ds_read_b64_tr_b16 a[70:71], v19 offset:50176\n"
"v_add_u32 v3, s52, v3\n"
"v_mfma_f32_16x16x32_bf16 v[172:175], a[24:27], a[12:15], v[172:175]\n"
"ds_read_b64_tr_b16 a[72:73], v20 offset:49152\n"
"v_lshlrev_b32 v3, 10, v3\n"
"v_mfma_f32_16x16x32_bf16 v[176:179], a[28:31], a[0:3], v[176:179]\n"
"ds_read_b64_tr_b16 a[74:75], v20 offset:50176\n"
"s_lshl_b32 s52, s49, 7\n"
"v_mfma_f32_16x16x32_bf16 v[180:183], a[28:31], a[4:7], v[180:183]\n"
"buffer_load_dwordx4 v11, s[12:15], s24 offen offset:3072 lds\n"
"v_add_u32 v3, s52, v3\n"
"v_mfma_f32_16x16x32_bf16 v[184:187], a[28:31], a[8:11], v[184:187]\n"
"ds_read_b64_tr_b16 a[76:77], v21 offset:49152\n"
"v_lshlrev_b32 v4, 1, v1\n"
"v_mfma_f32_16x16x32_bf16 v[188:191], a[28:31], a[12:15], v[188:191]\n"
"ds_read_b64_tr_b16 a[78:79], v21 offset:50176\n"
"s_add_u32 m0, s26, 0x8000\n"
"v_mfma_f32_16x16x32_bf16 v[192:195], a[32:35], a[0:3], v[192:195]\n"
"ds_read_b64_tr_b16 a[80:81], v18 offset:57344\n"
"s_mov_b32 s24, 0xc4000\n"
"v_mfma_f32_16x16x32_bf16 v[196:199], a[32:35], a[4:7], v[196:199]\n"
"buffer_load_dwordx4 v8, s[12:15], s24 offen offset:0 lds\n"
"s_waitcnt lgkmcnt(9)\n"
"v_mfma_f32_16x16x32_bf16 v[200:203], a[32:35], a[8:11], v[200:203]\n"
"ds_read_b64_tr_b16 a[82:83], v18 offset:58368\n"
"ds_read_b128 v[92:95], v53 offset:0\n"
"v_mfma_f32_16x16x32_bf16 v[204:207], a[32:35], a[12:15], v[204:207]\n"
"ds_read_b64_tr_b16 a[84:85], v19 offset:57344\n"
"ds_read_b128 v[96:99], v53 offset:16\n"
"v_mfma_f32_16x16x32_bf16 v[208:211], a[36:39], a[0:3], v[208:211]\n"
"ds_read_b64_tr_b16 a[86:87], v19 offset:58368\n"
"ds_read_b128 v[100:103], v53 offset:32\n"
"v_mfma_f32_16x16x32_bf16 v[212:215], a[36:39], a[4:7], v[212:215]\n"
"buffer_load_dwordx4 v9, s[12:15], s24 offen offset:1024 lds\n"
"ds_read_b128 v[104:107], v53 offset:48\n"
"v_mfma_f32_16x16x32_bf16 v[216:219], a[36:39], a[8:11], v[216:219]\n"
"ds_read_b64_tr_b16 a[88:89], v20 offset:57344\n"
"v_and_b32 v5, 7, v0\n"
"v_mfma_f32_16x16x32_bf16 v[220:223], a[36:39], a[12:15], v[220:223]\n"
"ds_read_b64_tr_b16 a[90:91], v20 offset:58368\n"
"v_xor_b32 v4, v4, v5\n"
"v_mfma_f32_16x16x32_bf16 v[224:227], a[40:43], a[0:3], v[224:227]\n"
"ds_read_b64_tr_b16 a[92:93], v21 offset:57344\n"
"v_lshl_add_u32 v30, v4, 4, v3\n"
"v_mfma_f32_16x16x32_bf16 v[228:231], a[40:43], a[4:7], v[228:231]\n"
"buffer_load_dwordx4 v10, s[12:15], s24 offen offset:2048 lds\n"
"v_add_u32 v31, 0x1c00, v30\n"
"v_mfma_f32_16x16x32_bf16 v[232:235], a[40:43], a[8:11], v[232:235]\n"
"ds_read_b64_tr_b16 a[94:95], v21 offset:58368\n"
"v_add_u32 v32, 0x3800, v30\n"
"v_mfma_f32_16x16x32_bf16 v[236:239], a[40:43], a[12:15], v[236:239]\n"
"v_add_u32 v33, 0x5400, v30\n"
"s_lshr_b32 s52, s6, 5\n"
"v_mfma_f32_16x16x32_bf16 v[240:243], a[44:47], a[0:3], v[240:243]\n"
"s_lshl_b32 s52, s52, 11\n"
"s_bfe_u32 s53, s6, 0x20003\n"
"v_mfma_f32_16x16x32_bf16 v[244:247], a[44:47], a[4:7], v[244:247]\n"
"buffer_load_dwordx4 v11, s[12:15], s24 offen offset:3072 lds\n"
"s_lshl_b32 s53, s53, 3\n"
"v_mfma_f32_16x16x32_bf16 v[248:251], a[44:47], a[8:11], v[248:251]\n"
"s_add_u32 s52, s52, s53\n"
"s_add_u32 s52, s52, 0xc000\n"
"v_mfma_f32_16x16x32_bf16 v[252:255], a[44:47], a[12:15], v[252:255]\n"
"s_waitcnt vmcnt(16) lgkmcnt(0)\n"
"s_barrier\n"
"v_mfma_f32_16x16x32_bf16 v[128:131], a[64:67], a[48:51], v[128:131]\n"
"ds_read_b64_tr_b16 a[16:17], v22 offset:0\n"
"s_add_u32 m0, s27, 0x0\n"
"s_movk_i32 s25, 0x200\n"
"v_mfma_f32_16x16x32_bf16 v[132:135], a[64:67], a[52:55], v[132:135]\n"
"buffer_load_dwordx4 v12, s[8:11], s25 offen offset:0 lds\n"
"s_mov_b32 s58, 0x05040100\n"
"v_mfma_f32_16x16x32_bf16 v[136:139], a[64:67], a[56:59], v[136:139]\n"
"ds_read_b64_tr_b16 a[18:19], v22 offset:1024\n"
"s_mov_b32 s59, 0x07060302\n"
"v_mfma_f32_16x16x32_bf16 v[140:143], a[64:67], a[60:63], v[140:143]\n"
"ds_read_b128 a[0:3], v16 offset:32768\n"
"s_waitcnt vmcnt(25)\n"
"v_mfma_f32_16x16x32_bf16 v[144:147], a[68:71], a[48:51], v[144:147]\n"
"ds_read_b128 a[4:7], v16 offset:34816\n"
"s_waitcnt lgkmcnt(8)\n"
"v_mfma_f32_16x16x32_bf16 v[148:151], a[68:71], a[52:55], v[148:151]\n"
"buffer_load_dwordx4 v13, s[8:11], s25 offen offset:1024 lds\n"
"v_perm_b32 v108, v72, v76, s58\n"
"v_mfma_f32_16x16x32_bf16 v[152:155], a[68:71], a[56:59], v[152:155]\n"
"ds_read_b128 a[8:11], v16 offset:36864\n"
"v_perm_b32 v109, v72, v76, s59\n"
"v_mfma_f32_16x16x32_bf16 v[156:159], a[68:71], a[60:63], v[156:159]\n"
"ds_read_b128 a[12:15], v16 offset:38912\n"
"v_perm_b32 v110, v73, v77, s58\n"
"v_mfma_f32_16x16x32_bf16 v[160:163], a[72:75], a[48:51], v[160:163]\n"
"ds_read_b64_tr_b16 a[20:21], v23 offset:0\n"
"v_perm_b32 v111, v73, v77, s59\n"
"v_mfma_f32_16x16x32_bf16 v[164:167], a[72:75], a[52:55], v[164:167]\n"
"buffer_load_dwordx4 v14, s[8:11], s25 offen offset:2048 lds\n"
"v_perm_b32 v112, v74, v78, s58\n"
"v_mfma_f32_16x16x32_bf16 v[168:171], a[72:75], a[56:59], v[168:171]\n"
"ds_read_b64_tr_b16 a[22:23], v23 offset:1024\n"
"v_perm_b32 v113, v74, v78, s59\n"
"v_mfma_f32_16x16x32_bf16 v[172:175], a[72:75], a[60:63], v[172:175]\n"
"ds_read_b64_tr_b16 a[24:25], v24 offset:0\n"
"v_perm_b32 v114, v75, v79, s58\n"
"v_mfma_f32_16x16x32_bf16 v[176:179], a[76:79], a[48:51], v[176:179]\n"
"ds_read_b64_tr_b16 a[26:27], v24 offset:1024\n"
"v_perm_b32 v115, v75, v79, s59\n"
"v_mfma_f32_16x16x32_bf16 v[180:183], a[76:79], a[52:55], v[180:183]\n"
"buffer_load_dwordx4 v15, s[8:11], s25 offen offset:3072 lds\n"
"v_perm_b32 v116, v64, v68, s58\n"
"v_mfma_f32_16x16x32_bf16 v[184:187], a[76:79], a[56:59], v[184:187]\n"
"ds_read_b64_tr_b16 a[28:29], v25 offset:0\n"
"v_perm_b32 v117, v64, v68, s59\n"
"v_mfma_f32_16x16x32_bf16 v[188:191], a[76:79], a[60:63], v[188:191]\n"
"ds_read_b64_tr_b16 a[30:31], v25 offset:1024\n"
"s_add_u32 m0, s26, 0xc000\n"
"v_mfma_f32_16x16x32_bf16 v[192:195], a[80:83], a[48:51], v[192:195]\n"
"ds_read_b64_tr_b16 a[32:33], v22 offset:8192\n"
"s_mov_b32 s24, 0xe0000\n"
"v_mfma_f32_16x16x32_bf16 v[196:199], a[80:83], a[52:55], v[196:199]\n"
"buffer_load_dwordx4 v8, s[12:15], s24 offen offset:0 lds\n"
"v_perm_b32 v118, v65, v69, s58\n"
"v_mfma_f32_16x16x32_bf16 v[200:203], a[80:83], a[56:59], v[200:203]\n"
"ds_read_b64_tr_b16 a[34:35], v22 offset:9216\n"
"v_perm_b32 v119, v65, v69, s59\n"
"v_mfma_f32_16x16x32_bf16 v[204:207], a[80:83], a[60:63], v[204:207]\n"
"ds_read_b64_tr_b16 a[36:37], v23 offset:8192\n"
"v_perm_b32 v120, v66, v70, s58\n"
"v_mfma_f32_16x16x32_bf16 v[208:211], a[84:87], a[48:51], v[208:211]\n"
"ds_read_b64_tr_b16 a[38:39], v23 offset:9216\n"
"v_perm_b32 v121, v66, v70, s59\n"
"v_mfma_f32_16x16x32_bf16 v[212:215], a[84:87], a[52:55], v[212:215]\n"
"buffer_load_dwordx4 v9, s[12:15], s24 offen offset:1024 lds\n"
"v_perm_b32 v122, v67, v71, s58\n"
"v_mfma_f32_16x16x32_bf16 v[216:219], a[84:87], a[56:59], v[216:219]\n"
"ds_read_b64_tr_b16 a[40:41], v24 offset:8192\n"
"v_perm_b32 v123, v67, v71, s59\n"
"v_mfma_f32_16x16x32_bf16 v[220:223], a[84:87], a[60:63], v[220:223]\n"
"ds_read_b64_tr_b16 a[42:43], v24 offset:9216\n"
"v_dot2_f32_bf16 v54, v108, v92, 0\n"
"v_dot2_f32_bf16 v55, v109, v94, 0\n"
"v_dot2_f32_bf16 v56, v110, v96, 0\n"
"v_dot2_f32_bf16 v57, v111, v98, 0\n"
"v_dot2_f32_bf16 v58, v112, v100, 0\n"
"v_dot2_f32_bf16 v59, v113, v102, 0\n"
"v_dot2_f32_bf16 v60, v114, v104, 0\n"
"v_dot2_f32_bf16 v61, v115, v106, 0\n"
"v_dot2c_f32_bf16 v54, v116, v93\n"
"v_dot2c_f32_bf16 v55, v117, v95\n"
"v_dot2c_f32_bf16 v56, v118, v97\n"
"v_dot2c_f32_bf16 v57, v119, v99\n"
"v_dot2c_f32_bf16 v58, v120, v101\n"
"v_dot2c_f32_bf16 v59, v121, v103\n"
"v_dot2c_f32_bf16 v60, v122, v105\n"
"v_dot2c_f32_bf16 v61, v123, v107\n"
"v_mfma_f32_16x16x32_bf16 v[224:227], a[88:91], a[48:51], v[224:227]\n"
"ds_read_b64_tr_b16 a[44:45], v25 offset:8192\n"
"v_lshlrev_b32 v108, 16, v84\n"
"v_mfma_f32_16x16x32_bf16 v[228:231], a[88:91], a[52:55], v[228:231]\n"
"buffer_load_dwordx4 v10, s[12:15], s24 offen offset:2048 lds\n"
"v_and_b32 v109, 0xffff0000, v84\n"
"v_mfma_f32_16x16x32_bf16 v[232:235], a[88:91], a[56:59], v[232:235]\n"
"ds_read_b64_tr_b16 a[46:47], v25 offset:9216\n"
"v_lshlrev_b32 v110, 16, v85\n"
"v_mfma_f32_16x16x32_bf16 v[236:239], a[88:91], a[60:63], v[236:239]\n"
"v_and_b32 v111, 0xffff0000, v85\n"
"v_lshlrev_b32 v112, 16, v86\n"
"v_mfma_f32_16x16x32_bf16 v[240:243], a[92:95], a[48:51], v[240:243]\n"
"v_and_b32 v113, 0xffff0000, v86\n"
"v_lshlrev_b32 v114, 16, v87\n"
"v_mfma_f32_16x16x32_bf16 v[244:247], a[92:95], a[52:55], v[244:247]\n"
"buffer_load_dwordx4 v11, s[12:15], s24 offen offset:3072 lds\n"
"v_and_b32 v115, 0xffff0000, v87\n"
"v_mfma_f32_16x16x32_bf16 v[248:251], a[92:95], a[56:59], v[248:251]\n"
"v_mul_f32 v54, v54, v108\n"
"v_mul_f32 v55, v55, v109\n"
"v_mfma_f32_16x16x32_bf16 v[252:255], a[92:95], a[60:63], v[252:255]\n"
"s_waitcnt vmcnt(20) lgkmcnt(0)\n"
"s_barrier\n"
"v_mfma_f32_16x16x32_bf16 v[128:131], a[16:19], a[0:3], v[128:131]\n"
"ds_read_b64_tr_b16 a[64:65], v18 offset:0\n"
"s_add_u32 m0, s26, 0x10000\n"
"s_mov_b32 s24, 0xfc000\n"
"v_mfma_f32_16x16x32_bf16 v[132:135], a[16:19], a[4:7], v[132:135]\n"
"buffer_load_dwordx4 v8, s[12:15], s24 offen offset:0 lds\n"
"v_mul_f32 v56, v56, v110\n"
"v_mfma_f32_16x16x32_bf16 v[136:139], a[16:19], a[8:11], v[136:139]\n"
"ds_read_b64_tr_b16 a[66:67], v18 offset:1024\n"
"v_mul_f32 v57, v57, v111\n"
"v_mfma_f32_16x16x32_bf16 v[140:143], a[16:19], a[12:15], v[140:143]\n"
"ds_read_b128 a[48:51], v17 offset:32768\n"
"v_mul_f32 v58, v58, v112\n"
"v_mfma_f32_16x16x32_bf16 v[144:147], a[20:23], a[0:3], v[144:147]\n"
"ds_read_b128 a[52:55], v17 offset:34816\n"
"v_mul_f32 v59, v59, v113\n"
"v_mfma_f32_16x16x32_bf16 v[148:151], a[20:23], a[4:7], v[148:151]\n"
"buffer_load_dwordx4 v9, s[12:15], s24 offen offset:1024 lds\n"
"v_mul_f32 v60, v60, v114\n"
"v_mfma_f32_16x16x32_bf16 v[152:155], a[20:23], a[8:11], v[152:155]\n"
"ds_read_b128 a[56:59], v17 offset:36864\n"
"v_mul_f32 v61, v61, v115\n"
"v_mfma_f32_16x16x32_bf16 v[156:159], a[20:23], a[12:15], v[156:159]\n"
"ds_read_b128 a[60:63], v17 offset:38912\n"
"v_lshrrev_b32 v51, 3, v0\n"
"v_mfma_f32_16x16x32_bf16 v[160:163], a[24:27], a[0:3], v[160:163]\n"
"ds_read_b64_tr_b16 a[68:69], v19 offset:0\n"
"s_lshl_b32 s28, s7, 3\n"
"v_mfma_f32_16x16x32_bf16 v[164:167], a[24:27], a[4:7], v[164:167]\n"
"buffer_load_dwordx4 v10, s[12:15], s24 offen offset:2048 lds\n"
"s_sub_u32 s28, 31, s28\n"
"v_mfma_f32_16x16x32_bf16 v[168:171], a[24:27], a[8:11], v[168:171]\n"
"ds_read_b64_tr_b16 a[70:71], v19 offset:1024\n"
"v_sub_u32 v51, s28, v51\n"
"v_mfma_f32_16x16x32_bf16 v[172:175], a[24:27], a[12:15], v[172:175]\n"
"ds_read_b64_tr_b16 a[72:73], v20 offset:0\n"
"v_and_b32 v53, 7, v0\n"
"v_mfma_f32_16x16x32_bf16 v[176:179], a[28:31], a[0:3], v[176:179]\n"
"ds_read_b64_tr_b16 a[74:75], v20 offset:1024\n"
"v_lshlrev_b32 v62, 2, v53\n"
"v_mfma_f32_16x16x32_bf16 v[180:183], a[28:31], a[4:7], v[180:183]\n"
"buffer_load_dwordx4 v11, s[12:15], s24 offen offset:3072 lds\n"
"v_xor_b32 v51, v51, v62\n"
"v_mfma_f32_16x16x32_bf16 v[184:187], a[28:31], a[8:11], v[184:187]\n"
"ds_read_b64_tr_b16 a[76:77], v21 offset:0\n"
"v_lshlrev_b32 v51, 2, v51\n"
"v_mfma_f32_16x16x32_bf16 v[188:191], a[28:31], a[12:15], v[188:191]\n"
"ds_read_b64_tr_b16 a[78:79], v21 offset:1024\n"
"s_add_u32 m0, s27, 0x4000\n"
"v_mfma_f32_16x16x32_bf16 v[192:195], a[32:35], a[0:3], v[192:195]\n"
"ds_read_b64_tr_b16 a[80:81], v18 offset:8192\n"
"s_movk_i32 s25, 0x280\n"
"v_mfma_f32_16x16x32_bf16 v[196:199], a[32:35], a[4:7], v[196:199]\n"
"buffer_load_dwordx4 v12, s[8:11], s25 offen offset:0 lds\n"
"v_lshl_add_u32 v51, v53, 10, v51\n"
"v_mfma_f32_16x16x32_bf16 v[200:203], a[32:35], a[8:11], v[200:203]\n"
"ds_read_b64_tr_b16 a[82:83], v18 offset:9216\n"
"v_add_u32 v51, 155648, v51\n"
"v_mfma_f32_16x16x32_bf16 v[204:207], a[32:35], a[12:15], v[204:207]\n"
"ds_read_b64_tr_b16 a[84:85], v19 offset:8192\n"
"v_perm_b32 v108, v76, v80, s58\n"
"v_mfma_f32_16x16x32_bf16 v[208:211], a[36:39], a[0:3], v[208:211]\n"
"ds_read_b64_tr_b16 a[86:87], v19 offset:9216\n"
"v_perm_b32 v109, v76, v80, s59\n"
"v_mfma_f32_16x16x32_bf16 v[212:215], a[36:39], a[4:7], v[212:215]\n"
"buffer_load_dwordx4 v13, s[8:11], s25 offen offset:1024 lds\n"
"v_perm_b32 v110, v77, v81, s58\n"
"v_mfma_f32_16x16x32_bf16 v[216:219], a[36:39], a[8:11], v[216:219]\n"
"ds_read_b64_tr_b16 a[88:89], v20 offset:8192\n"
"v_perm_b32 v111, v77, v81, s59\n"
"v_mfma_f32_16x16x32_bf16 v[220:223], a[36:39], a[12:15], v[220:223]\n"
"ds_read_b64_tr_b16 a[90:91], v20 offset:9216\n"
"v_perm_b32 v112, v78, v82, s58\n"
"v_mfma_f32_16x16x32_bf16 v[224:227], a[40:43], a[0:3], v[224:227]\n"
"ds_read_b64_tr_b16 a[92:93], v21 offset:8192\n"
"v_perm_b32 v113, v78, v82, s59\n"
"v_mfma_f32_16x16x32_bf16 v[228:231], a[40:43], a[4:7], v[228:231]\n"
"buffer_load_dwordx4 v14, s[8:11], s25 offen offset:2048 lds\n"
"v_perm_b32 v114, v79, v83, s58\n"
"v_mfma_f32_16x16x32_bf16 v[232:235], a[40:43], a[8:11], v[232:235]\n"
"ds_read_b64_tr_b16 a[94:95], v21 offset:9216\n"
"v_perm_b32 v115, v79, v83, s59\n"
"v_mfma_f32_16x16x32_bf16 v[236:239], a[40:43], a[12:15], v[236:239]\n"
"v_perm_b32 v116, v68, v72, s58\n"
"v_perm_b32 v117, v68, v72, s59\n"
"v_mfma_f32_16x16x32_bf16 v[240:243], a[44:47], a[0:3], v[240:243]\n"
"v_perm_b32 v118, v69, v73, s58\n"
"v_perm_b32 v119, v69, v73, s59\n"
"v_mfma_f32_16x16x32_bf16 v[244:247], a[44:47], a[4:7], v[244:247]\n"
"buffer_load_dwordx4 v15, s[8:11], s25 offen offset:3072 lds\n"
"v_perm_b32 v120, v70, v74, s58\n"
"v_mfma_f32_16x16x32_bf16 v[248:251], a[44:47], a[8:11], v[248:251]\n"
"v_perm_b32 v121, v70, v74, s59\n"
"v_perm_b32 v122, v71, v75, s58\n"
"v_mfma_f32_16x16x32_bf16 v[252:255], a[44:47], a[12:15], v[252:255]\n"
"s_waitcnt vmcnt(20) lgkmcnt(0)\n"
"s_barrier\n"
"v_mfma_f32_16x16x32_bf16 v[128:131], a[64:67], a[48:51], v[128:131]\n"
"ds_read_b64_tr_b16 a[16:17], v18 offset:16384\n"
"s_add_u32 m0, s26, 0x0\n"
"s_mov_b32 s24, 0x118000\n"
"v_mfma_f32_16x16x32_bf16 v[132:135], a[64:67], a[52:55], v[132:135]\n"
"buffer_load_dwordx4 v8, s[12:15], s24 offen offset:0 lds\n"
"s_lshl_b32 s22, s6, 13\n"
"v_mfma_f32_16x16x32_bf16 v[136:139], a[64:67], a[56:59], v[136:139]\n"
"ds_read_b64_tr_b16 a[18:19], v18 offset:17408\n"
"s_lshl_b32 s23, s7, 10\n"
"v_mfma_f32_16x16x32_bf16 v[140:143], a[64:67], a[60:63], v[140:143]\n"
"ds_read_b128 a[0:3], v16 offset:49152\n"
"s_add_u32 s23, s23, s22\n"
"v_mfma_f32_16x16x32_bf16 v[144:147], a[68:71], a[48:51], v[144:147]\n"
"ds_read_b128 a[4:7], v16 offset:51200\n"
"v_lshl_add_u32 v5, v0, 4, s23\n"
"v_mfma_f32_16x16x32_bf16 v[148:151], a[68:71], a[52:55], v[148:151]\n"
"buffer_load_dwordx4 v9, s[12:15], s24 offen offset:1024 lds\n"
"s_movk_i32 s55, 0x1000\n"
"v_mfma_f32_16x16x32_bf16 v[152:155], a[68:71], a[56:59], v[152:155]\n"
"ds_read_b128 a[8:11], v16 offset:53248\n"
"v_perm_b32 v123, v71, v75, s59\n"
"v_mfma_f32_16x16x32_bf16 v[156:159], a[68:71], a[60:63], v[156:159]\n"
"ds_read_b128 a[12:15], v16 offset:55296\n"
"v_dot2_f32_bf16 v64, v108, v92, 0\n"
"v_dot2_f32_bf16 v65, v109, v94, 0\n"
"v_dot2_f32_bf16 v66, v110, v96, 0\n"
"v_dot2_f32_bf16 v67, v111, v98, 0\n"
"v_dot2_f32_bf16 v68, v112, v100, 0\n"
"v_dot2_f32_bf16 v69, v113, v102, 0\n"
"v_dot2_f32_bf16 v70, v114, v104, 0\n"
"v_dot2_f32_bf16 v71, v115, v106, 0\n"
"v_dot2c_f32_bf16 v64, v116, v93\n"
"v_dot2c_f32_bf16 v65, v117, v95\n"
"v_dot2c_f32_bf16 v66, v118, v97\n"
"v_dot2c_f32_bf16 v67, v119, v99\n"
"v_dot2c_f32_bf16 v68, v120, v101\n"
"v_dot2c_f32_bf16 v69, v121, v103\n"
"v_dot2c_f32_bf16 v70, v122, v105\n"
"v_dot2c_f32_bf16 v71, v123, v107\n"
"v_mfma_f32_16x16x32_bf16 v[160:163], a[72:75], a[48:51], v[160:163]\n"
"ds_read_b64_tr_b16 a[20:21], v19 offset:16384\n"
"v_lshlrev_b32 v108, 16, v88\n"
"v_mfma_f32_16x16x32_bf16 v[164:167], a[72:75], a[52:55], v[164:167]\n"
"buffer_load_dwordx4 v10, s[12:15], s24 offen offset:2048 lds\n"
"v_and_b32 v109, 0xffff0000, v88\n"
"v_mfma_f32_16x16x32_bf16 v[168:171], a[72:75], a[56:59], v[168:171]\n"
"ds_read_b64_tr_b16 a[22:23], v19 offset:17408\n"
"v_lshlrev_b32 v110, 16, v89\n"
"v_mfma_f32_16x16x32_bf16 v[172:175], a[72:75], a[60:63], v[172:175]\n"
"ds_read_b64_tr_b16 a[24:25], v20 offset:16384\n"
"v_and_b32 v111, 0xffff0000, v89\n"
"v_mfma_f32_16x16x32_bf16 v[176:179], a[76:79], a[48:51], v[176:179]\n"
"ds_read_b64_tr_b16 a[26:27], v20 offset:17408\n"
"v_lshlrev_b32 v112, 16, v90\n"
"v_mfma_f32_16x16x32_bf16 v[180:183], a[76:79], a[52:55], v[180:183]\n"
"buffer_load_dwordx4 v11, s[12:15], s24 offen offset:3072 lds\n"
"v_and_b32 v113, 0xffff0000, v90\n"
"v_mfma_f32_16x16x32_bf16 v[184:187], a[76:79], a[56:59], v[184:187]\n"
"ds_read_b64_tr_b16 a[28:29], v21 offset:16384\n"
"v_lshlrev_b32 v114, 16, v91\n"
"v_mfma_f32_16x16x32_bf16 v[188:191], a[76:79], a[60:63], v[188:191]\n"
"ds_read_b64_tr_b16 a[30:31], v21 offset:17408\n"
"v_and_b32 v115, 0xffff0000, v91\n"
"v_mfma_f32_16x16x32_bf16 v[192:195], a[80:83], a[48:51], v[192:195]\n"
"ds_read_b64_tr_b16 a[32:33], v18 offset:24576\n"
"v_mul_f32 v64, v64, v108\n"
"v_mfma_f32_16x16x32_bf16 v[196:199], a[80:83], a[52:55], v[196:199]\n"
"v_mul_f32 v65, v65, v109\n"
"v_mul_f32 v66, v66, v110\n"
"v_mfma_f32_16x16x32_bf16 v[200:203], a[80:83], a[56:59], v[200:203]\n"
"ds_read_b64_tr_b16 a[34:35], v18 offset:25600\n"
"v_mul_f32 v67, v67, v111\n"
"v_mfma_f32_16x16x32_bf16 v[204:207], a[80:83], a[60:63], v[204:207]\n"
"ds_read_b64_tr_b16 a[36:37], v19 offset:24576\n"
"v_mul_f32 v68, v68, v112\n"
"v_mfma_f32_16x16x32_bf16 v[208:211], a[84:87], a[48:51], v[208:211]\n"
"ds_read_b64_tr_b16 a[38:39], v19 offset:25600\n"
"v_mul_f32 v69, v69, v113\n"
"v_mfma_f32_16x16x32_bf16 v[212:215], a[84:87], a[52:55], v[212:215]\n"
"v_mul_f32 v70, v70, v114\n"
"v_mul_f32 v71, v71, v115\n"
"v_mfma_f32_16x16x32_bf16 v[216:219], a[84:87], a[56:59], v[216:219]\n"
"ds_read_b64_tr_b16 a[40:41], v20 offset:24576\n"
"v_cvt_pk_bf16_f32 v108, v64, v54\n"
"v_mfma_f32_16x16x32_bf16 v[220:223], a[84:87], a[60:63], v[220:223]\n"
"ds_read_b64_tr_b16 a[42:43], v20 offset:25600\n"
"ds_write_b32 v51, v108 offset:0\n"
"v_mfma_f32_16x16x32_bf16 v[224:227], a[88:91], a[48:51], v[224:227]\n"
"ds_read_b64_tr_b16 a[44:45], v21 offset:24576\n"
"v_cvt_pk_bf16_f32 v109, v65, v55\n"
"v_mfma_f32_16x16x32_bf16 v[228:231], a[88:91], a[52:55], v[228:231]\n"
"ds_write_b32 v51, v109 offset:128\n"
"v_cvt_pk_bf16_f32 v110, v66, v56\n"
"v_mfma_f32_16x16x32_bf16 v[232:235], a[88:91], a[56:59], v[232:235]\n"
"ds_read_b64_tr_b16 a[46:47], v21 offset:25600\n"
"ds_write_b32 v51, v110 offset:256\n"
"v_mfma_f32_16x16x32_bf16 v[236:239], a[88:91], a[60:63], v[236:239]\n"
"v_cvt_pk_bf16_f32 v111, v67, v57\n"
"ds_write_b32 v51, v111 offset:384\n"
"v_mfma_f32_16x16x32_bf16 v[240:243], a[92:95], a[48:51], v[240:243]\n"
"v_cvt_pk_bf16_f32 v112, v68, v58\n"
"ds_write_b32 v51, v112 offset:512\n"
"v_mfma_f32_16x16x32_bf16 v[244:247], a[92:95], a[52:55], v[244:247]\n"
"v_cvt_pk_bf16_f32 v113, v69, v59\n"
"ds_write_b32 v51, v113 offset:640\n"
"v_mfma_f32_16x16x32_bf16 v[248:251], a[92:95], a[56:59], v[248:251]\n"
"v_cvt_pk_bf16_f32 v114, v70, v60\n"
"ds_write_b32 v51, v114 offset:768\n"
"v_mfma_f32_16x16x32_bf16 v[252:255], a[92:95], a[60:63], v[252:255]\n"
"s_waitcnt vmcnt(20) lgkmcnt(0)\n"
"s_barrier\n"
"v_mfma_f32_16x16x32_bf16 v[128:131], a[16:19], a[0:3], v[128:131]\n"
"ds_read_b64_tr_b16 a[64:65], v18 offset:32768\n"
"s_add_u32 m0, s26, 0x4000\n"
"s_mov_b32 s24, 0x134000\n"
"v_mfma_f32_16x16x32_bf16 v[132:135], a[16:19], a[4:7], v[132:135]\n"
"buffer_load_dwordx4 v8, s[12:15], s24 offen offset:0 lds\n"
"v_cvt_pk_bf16_f32 v115, v71, v61\n"
"v_mfma_f32_16x16x32_bf16 v[136:139], a[16:19], a[8:11], v[136:139]\n"
"ds_read_b64_tr_b16 a[66:67], v18 offset:33792\n"
"ds_write_b32 v51, v115 offset:896\n"
"v_mfma_f32_16x16x32_bf16 v[140:143], a[16:19], a[12:15], v[140:143]\n"
"ds_read_b128 a[48:51], v17 offset:49152\n"
"v_and_b32 v126, 31, v0\n"
"v_mfma_f32_16x16x32_bf16 v[144:147], a[20:23], a[0:3], v[144:147]\n"
"ds_read_b128 a[52:55], v17 offset:51200\n"
"v_lshl_add_u32 v126, v126, 6, s52\n"
"v_mfma_f32_16x16x32_bf16 v[148:151], a[20:23], a[4:7], v[148:151]\n"
"buffer_load_dwordx4 v9, s[12:15], s24 offen offset:1024 lds\n"
"s_add_u32 s5, s7, s49\n"
"v_mfma_f32_16x16x32_bf16 v[152:155], a[20:23], a[8:11], v[152:155]\n"
"ds_read_b128 a[56:59], v17 offset:53248\n"
"s_and_b32 s5, s5, 3\n"
"v_mfma_f32_16x16x32_bf16 v[156:159], a[20:23], a[12:15], v[156:159]\n"
"ds_read_b128 a[60:63], v17 offset:55296\n"
"s_lshl_b32 s52, s48, 6\n"
"v_mfma_f32_16x16x32_bf16 v[160:163], a[24:27], a[0:3], v[160:163]\n"
"ds_read_b64_tr_b16 a[68:69], v19 offset:32768\n"
"s_lshl_b32 s53, s5, 3\n"
"v_mfma_f32_16x16x32_bf16 v[164:167], a[24:27], a[4:7], v[164:167]\n"
"buffer_load_dwordx4 v10, s[12:15], s24 offen offset:2048 lds\n"
"s_add_u32 s52, s52, s53\n"
"v_mfma_f32_16x16x32_bf16 v[168:171], a[24:27], a[8:11], v[168:171]\n"
"ds_read_b64_tr_b16 a[70:71], v19 offset:33792\n"
"s_add_u32 s52, s52, 0x1000\n"
"v_mfma_f32_16x16x32_bf16 v[172:175], a[24:27], a[12:15], v[172:175]\n"
"ds_read_b64_tr_b16 a[72:73], v20 offset:32768\n"
"v_mov_b32 v4, s52\n"
"v_mfma_f32_16x16x32_bf16 v[176:179], a[28:31], a[0:3], v[176:179]\n"
"ds_read_b64_tr_b16 a[74:75], v20 offset:33792\n"
"v_cmp_lt_u32 vcc, 31, v0\n"
"v_mfma_f32_16x16x32_bf16 v[180:183], a[28:31], a[4:7], v[180:183]\n"
"buffer_load_dwordx4 v11, s[12:15], s24 offen offset:3072 lds\n"
"v_cndmask_b32 v126, v126, v4, vcc\n"
"v_mfma_f32_16x16x32_bf16 v[184:187], a[28:31], a[8:11], v[184:187]\n"
"ds_read_b64_tr_b16 a[76:77], v21 offset:32768\n"
"s_lshr_b32 s52, s7, 1\n"
"v_mfma_f32_16x16x32_bf16 v[188:191], a[28:31], a[12:15], v[188:191]\n"
"ds_read_b64_tr_b16 a[78:79], v21 offset:33792\n"
"s_add_u32 m0, s27, 0x8000\n"
"v_mfma_f32_16x16x32_bf16 v[192:195], a[32:35], a[0:3], v[192:195]\n"
"ds_read_b64_tr_b16 a[80:81], v18 offset:40960\n"
"s_movk_i32 s25, 0x300\n"
"v_mfma_f32_16x16x32_bf16 v[196:199], a[32:35], a[4:7], v[196:199]\n"
"buffer_load_dwordx4 v12, s[8:11], s25 offen offset:0 lds\n"
"s_mul_i32 s30, s52, 131328\n"
"v_mfma_f32_16x16x32_bf16 v[200:203], a[32:35], a[8:11], v[200:203]\n"
"ds_read_b64_tr_b16 a[82:83], v18 offset:41984\n"
"s_mul_i32 s52, s52, 4608\n"
"v_mfma_f32_16x16x32_bf16 v[204:207], a[32:35], a[12:15], v[204:207]\n"
"ds_read_b64_tr_b16 a[84:85], v19 offset:40960\n"
"s_add_u32 s87, s52, 147968\n"
"v_mfma_f32_16x16x32_bf16 v[208:211], a[36:39], a[0:3], v[208:211]\n"
"ds_read_b64_tr_b16 a[86:87], v19 offset:41984\n"
"s_and_b32 s53, s7, 1\n"
"v_mfma_f32_16x16x32_bf16 v[212:215], a[36:39], a[4:7], v[212:215]\n"
"buffer_load_dwordx4 v13, s[8:11], s25 offen offset:1024 lds\n"
"s_lshl_b32 s53, s53, 8\n"
"v_mfma_f32_16x16x32_bf16 v[216:219], a[36:39], a[8:11], v[216:219]\n"
"ds_read_b64_tr_b16 a[88:89], v20 offset:40960\n"
"s_add_u32 s52, s30, s53\n"
"v_mfma_f32_16x16x32_bf16 v[220:223], a[36:39], a[12:15], v[220:223]\n"
"ds_read_b64_tr_b16 a[90:91], v20 offset:41984\n"
"v_and_b32 v34, 3, v0\n"
"v_mfma_f32_16x16x32_bf16 v[224:227], a[40:43], a[0:3], v[224:227]\n"
"ds_read_b64_tr_b16 a[92:93], v21 offset:40960\n"
"v_mul_u32_u24 v124, 4160, v34\n"
"v_mfma_f32_16x16x32_bf16 v[228:231], a[40:43], a[4:7], v[228:231]\n"
"buffer_load_dwordx4 v[36:39], v5, s[36:39], 0 offen\n"
"v_lshl_add_u32 v34, v1, 4, v124\n"
"v_mfma_f32_16x16x32_bf16 v[232:235], a[40:43], a[8:11], v[232:235]\n"
"ds_read_b64_tr_b16 a[94:95], v21 offset:41984\n"
"v_bfe_u32 v4, v0, 2, 2\n"
"v_mfma_f32_16x16x32_bf16 v[236:239], a[40:43], a[12:15], v[236:239]\n"
"v_lshl_add_u32 v34, v4, 3, v34\n"
"v_add_u32 v34, s52, v34\n"
"v_mfma_f32_16x16x32_bf16 v[240:243], a[44:47], a[0:3], v[240:243]\n"
"s_add_u32 s53, s53, s87\n"
"s_add_u32 s53, s53, 480\n"
"v_mfma_f32_16x16x32_bf16 v[244:247], a[44:47], a[4:7], v[244:247]\n"
"buffer_load_dwordx2 v[40:41], v5, s[36:39], 0 offen offset:16\n"
"v_lshlrev_b32 v35, 5, v2\n"
"v_mfma_f32_16x16x32_bf16 v[248:251], a[44:47], a[8:11], v[248:251]\n"
"v_sub_u32 v35, s53, v35\n"
"v_lshl_add_u32 v35, v1, 4, v35\n"
"v_mfma_f32_16x16x32_bf16 v[252:255], a[44:47], a[12:15], v[252:255]\n"
"s_waitcnt vmcnt(20) lgkmcnt(0)\n"
"s_barrier\n"
"v_mfma_f32_16x16x32_bf16 v[128:131], a[64:67], a[48:51], v[128:131]\n"
"ds_read_b64_tr_b16 a[16:17], v18 offset:49152\n"
"v_bfe_u32 v4, v0, 2, 1\n"
"v_mfma_f32_16x16x32_bf16 v[132:135], a[64:67], a[52:55], v[132:135]\n"
"buffer_load_dwordx4 v14, s[8:11], s25 offen offset:2048 lds\n"
"v_lshl_add_u32 v124, v4, 4, v124\n"
"v_mfma_f32_16x16x32_bf16 v[136:139], a[64:67], a[56:59], v[136:139]\n"
"ds_read_b64_tr_b16 a[18:19], v18 offset:50176\n"
"v_add_u32 v124, s30, v124\n"
"v_mfma_f32_16x16x32_bf16 v[140:143], a[64:67], a[60:63], v[140:143]\n"
"ds_read_b128 a[0:3], v16 offset:0\n"
"s_lshl_b32 s52, s7, 10\n"
"v_mfma_f32_16x16x32_bf16 v[144:147], a[68:71], a[48:51], v[144:147]\n"
"ds_read_b128 a[4:7], v16 offset:2048\n"
"s_add_u32 s52, s52, 32\n"
"v_mfma_f32_16x16x32_bf16 v[148:151], a[68:71], a[52:55], v[148:151]\n"
"buffer_load_dwordx4 v15, s[8:11], s25 offen offset:3072 lds\n"
"v_lshl_add_u32 v125, v0, 4, s52\n"
"v_mfma_f32_16x16x32_bf16 v[152:155], a[68:71], a[56:59], v[152:155]\n"
"ds_read_b128 a[8:11], v16 offset:4096\n"
"v_lshlrev_b32 v52, 4, v0\n"
"v_mfma_f32_16x16x32_bf16 v[156:159], a[68:71], a[60:63], v[156:159]\n"
"ds_read_b128 a[12:15], v16 offset:6144\n"
"s_add_u32 m0, s26, 0x8000\n"
"v_mfma_f32_16x16x32_bf16 v[160:163], a[72:75], a[48:51], v[160:163]\n"
"ds_read_b64_tr_b16 a[20:21], v19 offset:49152\n"
"s_mov_b32 s24, 0x150000\n"
"v_mfma_f32_16x16x32_bf16 v[164:167], a[72:75], a[52:55], v[164:167]\n"
"buffer_load_dwordx4 v8, s[12:15], s24 offen offset:0 lds\n"
"v_mfma_f32_16x16x32_bf16 v[168:171], a[72:75], a[56:59], v[168:171]\n"
"ds_read_b64_tr_b16 a[22:23], v19 offset:50176\n"
"v_mfma_f32_16x16x32_bf16 v[172:175], a[72:75], a[60:63], v[172:175]\n"
"ds_read_b64_tr_b16 a[24:25], v20 offset:49152\n"
"v_mfma_f32_16x16x32_bf16 v[176:179], a[76:79], a[48:51], v[176:179]\n"
"ds_read_b64_tr_b16 a[26:27], v20 offset:50176\n"
"v_mfma_f32_16x16x32_bf16 v[180:183], a[76:79], a[52:55], v[180:183]\n"
"buffer_load_dwordx4 v9, s[12:15], s24 offen offset:1024 lds\n"
"v_mfma_f32_16x16x32_bf16 v[184:187], a[76:79], a[56:59], v[184:187]\n"
"ds_read_b64_tr_b16 a[28:29], v21 offset:49152\n"
"v_mfma_f32_16x16x32_bf16 v[188:191], a[76:79], a[60:63], v[188:191]\n"
"ds_read_b64_tr_b16 a[30:31], v21 offset:50176\n"
"v_mfma_f32_16x16x32_bf16 v[192:195], a[80:83], a[48:51], v[192:195]\n"
"ds_read_b64_tr_b16 a[32:33], v18 offset:57344\n"
"v_mfma_f32_16x16x32_bf16 v[196:199], a[80:83], a[52:55], v[196:199]\n"
"buffer_load_dwordx4 v10, s[12:15], s24 offen offset:2048 lds\n"
"v_mfma_f32_16x16x32_bf16 v[200:203], a[80:83], a[56:59], v[200:203]\n"
"ds_read_b64_tr_b16 a[34:35], v18 offset:58368\n"
"v_mfma_f32_16x16x32_bf16 v[204:207], a[80:83], a[60:63], v[204:207]\n"
"ds_read_b64_tr_b16 a[36:37], v19 offset:57344\n"
"v_mfma_f32_16x16x32_bf16 v[208:211], a[84:87], a[48:51], v[208:211]\n"
"ds_read_b64_tr_b16 a[38:39], v19 offset:58368\n"
"v_mfma_f32_16x16x32_bf16 v[212:215], a[84:87], a[52:55], v[212:215]\n"
"buffer_load_dwordx4 v11, s[12:15], s24 offen offset:3072 lds\n"
"v_mfma_f32_16x16x32_bf16 v[216:219], a[84:87], a[56:59], v[216:219]\n"
"ds_read_b64_tr_b16 a[40:41], v20 offset:57344\n"
"v_mfma_f32_16x16x32_bf16 v[220:223], a[84:87], a[60:63], v[220:223]\n"
"ds_read_b64_tr_b16 a[42:43], v20 offset:58368\n"
"v_mfma_f32_16x16x32_bf16 v[224:227], a[88:91], a[48:51], v[224:227]\n"
"ds_read_b64_tr_b16 a[44:45], v21 offset:57344\n"
"v_mfma_f32_16x16x32_bf16 v[228:231], a[88:91], a[52:55], v[228:231]\n"
"buffer_load_dwordx4 v[42:45], v5, s[36:39], s55 offen\n"
"v_mfma_f32_16x16x32_bf16 v[232:235], a[88:91], a[56:59], v[232:235]\n"
"ds_read_b64_tr_b16 a[46:47], v21 offset:58368\n"
"v_mfma_f32_16x16x32_bf16 v[236:239], a[88:91], a[60:63], v[236:239]\n"
"v_mfma_f32_16x16x32_bf16 v[240:243], a[92:95], a[48:51], v[240:243]\n"
"v_mfma_f32_16x16x32_bf16 v[244:247], a[92:95], a[52:55], v[244:247]\n"
"buffer_load_dwordx2 v[46:47], v5, s[36:39], s55 offen offset:16\n"
"s_lshr_b32 s23, s7, 1\n"
"v_mfma_f32_16x16x32_bf16 v[248:251], a[92:95], a[56:59], v[248:251]\n"
"s_lshl_b32 s23, s23, 12\n"
"s_add_u32 s23, s23, s22\n"
"v_mfma_f32_16x16x32_bf16 v[252:255], a[92:95], a[60:63], v[252:255]\n"
"s_waitcnt vmcnt(24) lgkmcnt(0)\n"
"s_barrier\n"
"v_mfma_f32_16x16x32_bf16 v[128:131], a[16:19], a[0:3], v[128:131]\n"
"ds_read_b64_tr_b16 a[64:65], v22 offset:0\n"
"s_add_u32 m0, s26, 0xc000\n"
"s_mov_b32 s24, 0x16c000\n"
"v_mfma_f32_16x16x32_bf16 v[132:135], a[16:19], a[4:7], v[132:135]\n"
"buffer_load_dwordx4 v8, s[12:15], s24 offen offset:0 lds\n"
"v_mov_b32 v28, s23\n"
"v_mfma_f32_16x16x32_bf16 v[136:139], a[16:19], a[8:11], v[136:139]\n"
"ds_read_b64_tr_b16 a[66:67], v22 offset:1024\n"
"v_mfma_f32_16x16x32_bf16 v[140:143], a[16:19], a[12:15], v[140:143]\n"
"ds_read_b128 a[48:51], v17 offset:0\n"
"v_mfma_f32_16x16x32_bf16 v[144:147], a[20:23], a[0:3], v[144:147]\n"
"ds_read_b128 a[52:55], v17 offset:2048\n"
"v_mfma_f32_16x16x32_bf16 v[148:151], a[20:23], a[4:7], v[148:151]\n"
"buffer_load_dwordx4 v9, s[12:15], s24 offen offset:1024 lds\n"
"v_mfma_f32_16x16x32_bf16 v[152:155], a[20:23], a[8:11], v[152:155]\n"
"ds_read_b128 a[56:59], v17 offset:4096\n"
"v_mfma_f32_16x16x32_bf16 v[156:159], a[20:23], a[12:15], v[156:159]\n"
"ds_read_b128 a[60:63], v17 offset:6144\n"
"v_mfma_f32_16x16x32_bf16 v[160:163], a[24:27], a[0:3], v[160:163]\n"
"ds_read_b64_tr_b16 a[68:69], v23 offset:0\n"
"v_mfma_f32_16x16x32_bf16 v[164:167], a[24:27], a[4:7], v[164:167]\n"
"buffer_load_dwordx4 v10, s[12:15], s24 offen offset:2048 lds\n"
"v_mfma_f32_16x16x32_bf16 v[168:171], a[24:27], a[8:11], v[168:171]\n"
"ds_read_b64_tr_b16 a[70:71], v23 offset:1024\n"
"v_mfma_f32_16x16x32_bf16 v[172:175], a[24:27], a[12:15], v[172:175]\n"
"ds_read_b64_tr_b16 a[72:73], v24 offset:0\n"
"v_mfma_f32_16x16x32_bf16 v[176:179], a[28:31], a[0:3], v[176:179]\n"
"ds_read_b64_tr_b16 a[74:75], v24 offset:1024\n"
"v_mfma_f32_16x16x32_bf16 v[180:183], a[28:31], a[4:7], v[180:183]\n"
"buffer_load_dwordx4 v11, s[12:15], s24 offen offset:3072 lds\n"
"v_mfma_f32_16x16x32_bf16 v[184:187], a[28:31], a[8:11], v[184:187]\n"
"ds_read_b64_tr_b16 a[76:77], v25 offset:0\n"
"v_mfma_f32_16x16x32_bf16 v[188:191], a[28:31], a[12:15], v[188:191]\n"
"ds_read_b64_tr_b16 a[78:79], v25 offset:1024\n"
"s_add_u32 m0, s27, 0xc000\n"
"v_mfma_f32_16x16x32_bf16 v[192:195], a[32:35], a[0:3], v[192:195]\n"
"ds_read_b64_tr_b16 a[80:81], v22 offset:8192\n"
"s_movk_i32 s25, 0x380\n"
"v_mfma_f32_16x16x32_bf16 v[196:199], a[32:35], a[4:7], v[196:199]\n"
"buffer_load_dwordx4 v12, s[8:11], s25 offen offset:0 lds\n"
"v_mfma_f32_16x16x32_bf16 v[200:203], a[32:35], a[8:11], v[200:203]\n"
"ds_read_b64_tr_b16 a[82:83], v22 offset:9216\n"
"v_mfma_f32_16x16x32_bf16 v[204:207], a[32:35], a[12:15], v[204:207]\n"
"ds_read_b64_tr_b16 a[84:85], v23 offset:8192\n"
"v_mfma_f32_16x16x32_bf16 v[208:211], a[36:39], a[0:3], v[208:211]\n"
"ds_read_b64_tr_b16 a[86:87], v23 offset:9216\n"
"v_mfma_f32_16x16x32_bf16 v[212:215], a[36:39], a[4:7], v[212:215]\n"
"buffer_load_dwordx4 v13, s[8:11], s25 offen offset:1024 lds\n"
"v_mfma_f32_16x16x32_bf16 v[216:219], a[36:39], a[8:11], v[216:219]\n"
"ds_read_b64_tr_b16 a[88:89], v24 offset:8192\n"
"v_mfma_f32_16x16x32_bf16 v[220:223], a[36:39], a[12:15], v[220:223]\n"
"ds_read_b64_tr_b16 a[90:91], v24 offset:9216\n"
"v_mfma_f32_16x16x32_bf16 v[224:227], a[40:43], a[0:3], v[224:227]\n"
"ds_read_b64_tr_b16 a[92:93], v25 offset:8192\n"
"v_mfma_f32_16x16x32_bf16 v[228:231], a[40:43], a[4:7], v[228:231]\n"
"buffer_load_dwordx2 v[26:27], v127, s[16:19], 0 offen sc1\n"
"v_mfma_f32_16x16x32_bf16 v[232:235], a[40:43], a[8:11], v[232:235]\n"
"ds_read_b64_tr_b16 a[94:95], v25 offset:9216\n"
"v_mfma_f32_16x16x32_bf16 v[236:239], a[40:43], a[12:15], v[236:239]\n"
"v_mfma_f32_16x16x32_bf16 v[240:243], a[44:47], a[0:3], v[240:243]\n"
"v_mfma_f32_16x16x32_bf16 v[244:247], a[44:47], a[4:7], v[244:247]\n"
"v_mfma_f32_16x16x32_bf16 v[248:251], a[44:47], a[8:11], v[248:251]\n"
"v_mfma_f32_16x16x32_bf16 v[252:255], a[44:47], a[12:15], v[252:255]\n"
"s_waitcnt vmcnt(23) lgkmcnt(0)\n"
"s_barrier\n"
"v_mfma_f32_16x16x32_bf16 v[128:131], a[64:67], a[48:51], v[128:131]\n"
"ds_read_b64_tr_b16 a[16:17], v18 offset:0\n"
"s_lshl_b32 s28, s7, 1\n"
"v_mfma_f32_16x16x32_bf16 v[132:135], a[64:67], a[52:55], v[132:135]\n"
"buffer_load_dwordx4 v14, s[8:11], s25 offen offset:2048 lds\n"
"v_and_b32 v53, 7, v0\n"
"v_mfma_f32_16x16x32_bf16 v[136:139], a[64:67], a[56:59], v[136:139]\n"
"ds_read_b64_tr_b16 a[18:19], v18 offset:1024\n"
"v_xor_b32 v51, s28, v53\n"
"v_mfma_f32_16x16x32_bf16 v[140:143], a[64:67], a[60:63], v[140:143]\n"
"ds_read_b128 a[0:3], v16 offset:16384\n"
"v_lshlrev_b32 v51, 4, v51\n"
"v_mfma_f32_16x16x32_bf16 v[144:147], a[68:71], a[48:51], v[144:147]\n"
"ds_read_b128 a[4:7], v16 offset:18432\n"
"v_lshrrev_b32 v62, 3, v0\n"
"v_mfma_f32_16x16x32_bf16 v[148:151], a[68:71], a[52:55], v[148:151]\n"
"buffer_load_dwordx4 v15, s[8:11], s25 offen offset:3072 lds\n"
"v_lshl_add_u32 v51, v62, 7, v51\n"
"v_mfma_f32_16x16x32_bf16 v[152:155], a[68:71], a[56:59], v[152:155]\n"
"ds_read_b128 a[8:11], v16 offset:20480\n"
"s_lshl_b32 s28, s7, 11\n"
"v_mfma_f32_16x16x32_bf16 v[156:159], a[68:71], a[60:63], v[156:159]\n"
"ds_read_b128 a[12:15], v16 offset:22528\n"
"s_add_u32 m0, s26, 0x10000\n"
"v_mfma_f32_16x16x32_bf16 v[160:163], a[72:75], a[48:51], v[160:163]\n"
"ds_read_b64_tr_b16 a[20:21], v19 offset:0\n"
"s_mov_b32 s24, 0x188000\n"
"v_mfma_f32_16x16x32_bf16 v[164:167], a[72:75], a[52:55], v[164:167]\n"
"buffer_load_dwordx4 v8, s[12:15], s24 offen offset:0 lds\n"
"s_add_u32 s28, s28, 155648\n"
"v_mfma_f32_16x16x32_bf16 v[168:171], a[72:75], a[56:59], v[168:171]\n"
"ds_read_b64_tr_b16 a[22:23], v19 offset:1024\n"
"v_add_u32 v51, s28, v51\n"
"v_mfma_f32_16x16x32_bf16 v[172:175], a[72:75], a[60:63], v[172:175]\n"
"ds_read_b64_tr_b16 a[24:25], v20 offset:0\n"
"ds_read_b128 v[54:57], v51\n"
"v_mfma_f32_16x16x32_bf16 v[176:179], a[76:79], a[48:51], v[176:179]\n"
"ds_read_b64_tr_b16 a[26:27], v20 offset:1024\n"
"v_xor_b32 v51, 16, v51\n"
"v_mfma_f32_16x16x32_bf16 v[180:183], a[76:79], a[52:55], v[180:183]\n"
"buffer_load_dwordx4 v9, s[12:15], s24 offen offset:1024 lds\n"
"ds_read_b128 v[58:61], v51 offset:1024\n"
"v_mfma_f32_16x16x32_bf16 v[184:187], a[76:79], a[56:59], v[184:187]\n"
"ds_read_b64_tr_b16 a[28:29], v21 offset:0\n"
"v_lshlrev_b32 v53, 4, v0\n"
"v_mfma_f32_16x16x32_bf16 v[188:191], a[76:79], a[60:63], v[188:191]\n"
"ds_read_b64_tr_b16 a[30:31], v21 offset:1024\n"
"s_lshl_b32 s28, s57, 13\n"
"v_mfma_f32_16x16x32_bf16 v[192:195], a[80:83], a[48:51], v[192:195]\n"
"ds_read_b64_tr_b16 a[32:33], v18 offset:8192\n"
"s_lshl_b32 s59, s20, 10\n"
"v_mfma_f32_16x16x32_bf16 v[196:199], a[80:83], a[52:55], v[196:199]\n"
"buffer_load_dwordx4 v10, s[12:15], s24 offen offset:2048 lds\n"
"s_lshr_b32 s4, s7, 1\n"
"v_mfma_f32_16x16x32_bf16 v[200:203], a[80:83], a[56:59], v[200:203]\n"
"ds_read_b64_tr_b16 a[34:35], v18 offset:9216\n"
"s_lshl_b32 s4, s4, 9\n"
"v_mfma_f32_16x16x32_bf16 v[204:207], a[80:83], a[60:63], v[204:207]\n"
"ds_read_b64_tr_b16 a[36:37], v19 offset:8192\n"
"s_add_u32 s59, s59, s4\n"
"v_mfma_f32_16x16x32_bf16 v[208:211], a[84:87], a[48:51], v[208:211]\n"
"ds_read_b64_tr_b16 a[38:39], v19 offset:9216\n"
"s_add_u32 s59, s59, 0x10000\n"
"v_mfma_f32_16x16x32_bf16 v[212:215], a[84:87], a[52:55], v[212:215]\n"
"buffer_load_dwordx4 v11, s[12:15], s24 offen offset:3072 lds\n"
"s_and_b32 s4, s7, 1\n"
"v_mfma_f32_16x16x32_bf16 v[216:219], a[84:87], a[56:59], v[216:219]\n"
"ds_read_b64_tr_b16 a[40:41], v20 offset:8192\n"
"s_lshl_b32 s4, s4, 8\n"
"v_mfma_f32_16x16x32_bf16 v[220:223], a[84:87], a[60:63], v[220:223]\n"
"ds_read_b64_tr_b16 a[42:43], v20 offset:9216\n"
"s_lshl_b32 s54, s21, 9\n"
"v_mfma_f32_16x16x32_bf16 v[224:227], a[88:91], a[48:51], v[224:227]\n"
"ds_read_b64_tr_b16 a[44:45], v21 offset:8192\n"
"s_add_u32 s4, s4, s54\n"
"v_mfma_f32_16x16x32_bf16 v[228:231], a[88:91], a[52:55], v[228:231]\n"
"buffer_load_dwordx2 v[48:49], v28, s[36:39], 0 offen\n"
"s_lshl_b32 s23, s6, 2\n"
"v_mfma_f32_16x16x32_bf16 v[232:235], a[88:91], a[56:59], v[232:235]\n"
"ds_read_b64_tr_b16 a[46:47], v21 offset:9216\n"
"s_and_b32 s55, s7, 2\n"
"v_mfma_f32_16x16x32_bf16 v[236:239], a[88:91], a[60:63], v[236:239]\n"
"s_add_u32 s23, s23, s55\n"
"v_mov_b32 v28, s23\n"
"v_mfma_f32_16x16x32_bf16 v[240:243], a[92:95], a[48:51], v[240:243]\n"
"s_add_u32 s4, s4, 0x14000\n"
"v_lshrrev_b32 v6, 5, v0\n"
"v_mfma_f32_16x16x32_bf16 v[244:247], a[92:95], a[52:55], v[244:247]\n"
"global_load_ushort v50, v28, s[50:51]\n"
"v_mul_u32_u24 v6, 6912, v6\n"
"v_mfma_f32_16x16x32_bf16 v[248:251], a[92:95], a[56:59], v[248:251]\n"
"v_lshlrev_b32 v7, 3, v0\n"
"v_add_u32 v6, v6, v7\n"
"v_mfma_f32_16x16x32_bf16 v[252:255], a[92:95], a[60:63], v[252:255]\n"
"s_waitcnt vmcnt(27) lgkmcnt(0)\n"
"s_barrier\n"
"v_mfma_f32_16x16x32_bf16 v[128:131], a[16:19], a[0:3], v[128:131]\n"
"ds_read_b64_tr_b16 a[64:65], v18 offset:16384\n"
"s_add_u32 m0, s26, 0x0\n"
"s_mov_b32 s24, 0x1a4000\n"
"v_mfma_f32_16x16x32_bf16 v[132:135], a[16:19], a[4:7], v[132:135]\n"
"buffer_load_dwordx4 v8, s[12:15], s24 offen offset:0 lds\n"
"s_lshl_b32 s29, s7, 11\n"
"v_mfma_f32_16x16x32_bf16 v[136:139], a[16:19], a[8:11], v[136:139]\n"
"ds_read_b64_tr_b16 a[66:67], v18 offset:17408\n"
"s_add_u32 s28, s28, s29\n"
"v_mfma_f32_16x16x32_bf16 v[140:143], a[16:19], a[12:15], v[140:143]\n"
"ds_read_b128 a[48:51], v17 offset:16384\n"
"s_mul_i32 s29, s56, 65792\n"
"v_mfma_f32_16x16x32_bf16 v[144:147], a[20:23], a[0:3], v[144:147]\n"
"ds_read_b128 a[52:55], v17 offset:18432\n"
"s_sub_u32 s28, s28, s29\n"
"v_mfma_f32_16x16x32_bf16 v[148:151], a[20:23], a[4:7], v[148:151]\n"
"buffer_load_dwordx4 v9, s[12:15], s24 offen offset:1024 lds\n"
"s_add_u32 s58, s28, 0x2f1f00\n"
"v_mfma_f32_16x16x32_bf16 v[152:155], a[20:23], a[8:11], v[152:155]\n"
"ds_read_b128 a[56:59], v17 offset:20480\n"
"s_lshl_b32 s22, s20, 3\n"
"v_mfma_f32_16x16x32_bf16 v[156:159], a[20:23], a[12:15], v[156:159]\n"
"ds_read_b128 a[60:63], v17 offset:22528\n"
"s_and_b32 s23, s7, 2\n"
"v_mfma_f32_16x16x32_bf16 v[160:163], a[24:27], a[0:3], v[160:163]\n"
"ds_read_b64_tr_b16 a[68:69], v19 offset:16384\n"
"s_lshl_b32 s23, s23, 1\n"
"v_mfma_f32_16x16x32_bf16 v[164:167], a[24:27], a[4:7], v[164:167]\n"
"buffer_load_dwordx4 v10, s[12:15], s24 offen offset:2048 lds\n"
"s_add_u32 s22, s22, s23\n"
"v_mfma_f32_16x16x32_bf16 v[168:171], a[24:27], a[8:11], v[168:171]\n"
"ds_read_b64_tr_b16 a[70:71], v19 offset:17408\n"
"s_mul_i32 s22, s22, 56\n"
"v_mfma_f32_16x16x32_bf16 v[172:175], a[24:27], a[12:15], v[172:175]\n"
"ds_read_b64_tr_b16 a[72:73], v20 offset:16384\n"
"s_lshl_b32 s23, s21, 2\n"
"v_mfma_f32_16x16x32_bf16 v[176:179], a[28:31], a[0:3], v[176:179]\n"
"ds_read_b64_tr_b16 a[74:75], v20 offset:17408\n"
"s_add_u32 s22, s22, s23\n"
"v_mfma_f32_16x16x32_bf16 v[180:183], a[28:31], a[4:7], v[180:183]\n"
"buffer_load_dwordx4 v11, s[12:15], s24 offen offset:3072 lds\n"
"s_and_b32 s23, s7, 1\n"
"v_mfma_f32_16x16x32_bf16 v[184:187], a[28:31], a[8:11], v[184:187]\n"
"ds_read_b64_tr_b16 a[76:77], v21 offset:16384\n"
"s_waitcnt vmcnt(12)\n"
"v_cmp_ne_u64 vcc, s[34:35], v[26:27]\n"
"s_nop 4\n"
"s_cbranch_vccnz .L_slow_st%=\n"
".L_back_st%=:\n"
"v_mfma_f32_16x16x32_bf16 v[188:191], a[28:31], a[12:15], v[188:191]\n"
"ds_read_b64_tr_b16 a[78:79], v21 offset:17408\n"
"s_lshl_b32 s23, s23, 1\n"
"v_mfma_f32_16x16x32_bf16 v[192:195], a[32:35], a[0:3], v[192:195]\n"
"ds_read_b64_tr_b16 a[80:81], v18 offset:24576\n"
"s_add_u32 s22, s22, s23\n"
"v_mfma_f32_16x16x32_bf16 v[196:199], a[32:35], a[4:7], v[196:199]\n"
"buffer_load_dwordx2 v[120:121], v6, s[16:19], s4 offen sc1\n"
"s_lshl_b32 s22, s22, 10\n"
"v_mfma_f32_16x16x32_bf16 v[200:203], a[32:35], a[8:11], v[200:203]\n"
"ds_read_b64_tr_b16 a[82:83], v18 offset:25600\n"
"s_add_u32 s52, s22, 0x580000\n"
"v_mfma_f32_16x16x32_bf16 v[204:207], a[32:35], a[12:15], v[204:207]\n"
"ds_read_b64_tr_b16 a[84:85], v19 offset:24576\n"
"s_add_u32 s53, s52, 57344\n"
"v_mfma_f32_16x16x32_bf16 v[208:211], a[36:39], a[0:3], v[208:211]\n"
"ds_read_b64_tr_b16 a[86:87], v19 offset:25600\n"
"s_add_u32 s54, s52, 114688\n"
"v_mfma_f32_16x16x32_bf16 v[212:215], a[36:39], a[4:7], v[212:215]\n"
"buffer_load_dwordx2 v[122:123], v7, s[16:19], s59 offen sc1\n"
"s_add_u32 s55, s52, 172032\n"
"v_mfma_f32_16x16x32_bf16 v[216:219], a[36:39], a[8:11], v[216:219]\n"
"ds_read_b64_tr_b16 a[88:89], v20 offset:24576\n"
"s_mul_i32 s22, s20, 14\n"
"v_mfma_f32_16x16x32_bf16 v[220:223], a[36:39], a[12:15], v[220:223]\n"
"ds_read_b64_tr_b16 a[90:91], v20 offset:25600\n"
"s_add_u32 s22, s22, s21\n"
"v_mfma_f32_16x16x32_bf16 v[224:227], a[40:43], a[0:3], v[224:227]\n"
"ds_read_b64_tr_b16 a[92:93], v21 offset:24576\n"
"s_lshl_b32 s22, s22, 6\n"
"v_mfma_f32_16x16x32_bf16 v[228:231], a[40:43], a[4:7], v[228:231]\n"
"s_waitcnt lgkmcnt(15)\n"
"buffer_store_dwordx4 v[54:57], v53, s[16:19], s58 offen sc1\n"
"v_mfma_f32_16x16x32_bf16 v[232:235], a[40:43], a[8:11], v[232:235]\n"
"ds_read_b64_tr_b16 a[94:95], v21 offset:25600\n"
"s_lshl_b32 s23, s7, 3\n"
"v_mfma_f32_16x16x32_bf16 v[236:239], a[40:43], a[12:15], v[236:239]\n"
"s_add_u32 s22, s22, s23\n"
"s_lshl_b32 s23, s21, 9\n"
"v_mfma_f32_16x16x32_bf16 v[240:243], a[44:47], a[0:3], v[240:243]\n"
"s_add_u32 s21, s22, 0x4000\n"
"v_mfma_f32_16x16x32_bf16 v[244:247], a[44:47], a[4:7], v[244:247]\n"
"buffer_store_dwordx4 v[58:61], v53, s[16:19], s58 offen offset:1024 sc1\n"
"s_lshl_b32 s28, s57, 11\n"
"v_mfma_f32_16x16x32_bf16 v[248:251], a[44:47], a[8:11], v[248:251]\n"
"s_lshl_b32 s29, s56, 6\n"
"s_add_u32 s28, s28, s29\n"
"v_mfma_f32_16x16x32_bf16 v[252:255], a[44:47], a[12:15], v[252:255]\n"
"s_waitcnt vmcnt(25) lgkmcnt(0)\n"
"s_barrier\n"
"v_mfma_f32_16x16x32_bf16 v[128:131], a[64:67], a[48:51], v[128:131]\n"
"ds_read_b64_tr_b16 a[16:17], v18 offset:32768\n"
"s_add_u32 m0, s26, 0x14100\n"
"s_add_u32 s24, s86, 0x0\n"
"s_cmp_ge_u32 s24, 0x70000\n"
"s_cselect_b32 s31, 0x70000, 0\n"
"s_sub_u32 s24, s24, s31\n"
"v_mfma_f32_16x16x32_bf16 v[132:135], a[64:67], a[52:55], v[132:135]\n"
"buffer_load_dwordx4 v30, s[40:43], s24 offen offset:0 lds\n"
"v_add_u32 v8, s26, v29\n"
"v_mfma_f32_16x16x32_bf16 v[136:139], a[64:67], a[56:59], v[136:139]\n"
"ds_read_b64_tr_b16 a[18:19], v18 offset:33792\n"
"v_xor_b32 v9, 32, v8\n"
"v_mfma_f32_16x16x32_bf16 v[140:143], a[64:67], a[60:63], v[140:143]\n"
"ds_read_b128 a[0:3], v16 offset:32768\n"
"v_xor_b32 v10, 64, v8\n"
"v_mfma_f32_16x16x32_bf16 v[144:147], a[68:71], a[48:51], v[144:147]\n"
"ds_read_b128 a[4:7], v16 offset:34816\n"
"v_xor_b32 v11, 0x60, v8\n"
"v_mfma_f32_16x16x32_bf16 v[148:151], a[68:71], a[52:55], v[148:151]\n"
"buffer_load_dwordx4 v31, s[40:43], s24 offen offset:1024 lds\n"
"v_add_u32 v12, 0x10000, v8\n"
"v_mfma_f32_16x16x32_bf16 v[152:155], a[68:71], a[56:59], v[152:155]\n"
"ds_read_b128 a[8:11], v16 offset:36864\n"
"v_add_u32 v13, 0x10000, v9\n"
"v_mfma_f32_16x16x32_bf16 v[156:159], a[68:71], a[60:63], v[156:159]\n"
"ds_read_b128 a[12:15], v16 offset:38912\n"
"v_add_u32 v14, 0x10000, v10\n"
"v_mfma_f32_16x16x32_bf16 v[160:163], a[72:75], a[48:51], v[160:163]\n"
"ds_read_b64_tr_b16 a[20:21], v19 offset:32768\n"
"v_add_u32 v15, 0x10000, v11\n"
"v_mfma_f32_16x16x32_bf16 v[164:167], a[72:75], a[52:55], v[164:167]\n"
"buffer_load_dwordx4 v32, s[40:43], s24 offen offset:2048 lds\n"
"s_waitcnt vmcnt(11)\n"
"v_mfma_f32_16x16x32_bf16 v[168:171], a[72:75], a[56:59], v[168:171]\n"
"ds_read_b64_tr_b16 a[22:23], v19 offset:33792\n"
"v_alignbit_b32 v54, v37, v36, 16\n"
"v_mfma_f32_16x16x32_bf16 v[172:175], a[72:75], a[60:63], v[172:175]\n"
"ds_read_b64_tr_b16 a[24:25], v20 offset:32768\n"
"v_alignbit_b32 v55, v38, v37, 16\n"
"v_mfma_f32_16x16x32_bf16 v[176:179], a[76:79], a[48:51], v[176:179]\n"
"ds_read_b64_tr_b16 a[26:27], v20 offset:33792\n"
"v_alignbit_b32 v56, v39, v38, 16\n"
"v_mfma_f32_16x16x32_bf16 v[180:183], a[76:79], a[52:55], v[180:183]\n"
"buffer_load_dwordx4 v33, s[40:43], s24 offen offset:3072 lds\n"
"v_alignbit_b32 v57, v40, v39, 16\n"
"v_mfma_f32_16x16x32_bf16 v[184:187], a[76:79], a[56:59], v[184:187]\n"
"ds_read_b64_tr_b16 a[28:29], v21 offset:32768\n"
"v_mov_b32 v104, v37\n"
"v_mfma_f32_16x16x32_bf16 v[188:191], a[76:79], a[60:63], v[188:191]\n"
"ds_read_b64_tr_b16 a[30:31], v21 offset:33792\n"
"v_mov_b32 v105, v38\n"
"v_mfma_f32_16x16x32_bf16 v[192:195], a[80:83], a[48:51], v[192:195]\n"
"ds_read_b64_tr_b16 a[32:33], v18 offset:40960\n"
"v_mov_b32 v106, v39\n"
"v_mfma_f32_16x16x32_bf16 v[196:199], a[80:83], a[52:55], v[196:199]\n"
"v_mov_b32 v107, v40\n"
"v_alignbit_b32 v58, v38, v37, 16\n"
"v_mfma_f32_16x16x32_bf16 v[200:203], a[80:83], a[56:59], v[200:203]\n"
"ds_read_b64_tr_b16 a[34:35], v18 offset:41984\n"
"v_alignbit_b32 v59, v39, v38, 16\n"
"v_mfma_f32_16x16x32_bf16 v[204:207], a[80:83], a[60:63], v[204:207]\n"
"ds_read_b64_tr_b16 a[36:37], v19 offset:40960\n"
"v_alignbit_b32 v60, v40, v39, 16\n"
"v_mfma_f32_16x16x32_bf16 v[208:211], a[84:87], a[48:51], v[208:211]\n"
"ds_read_b64_tr_b16 a[38:39], v19 offset:41984\n"
"v_alignbit_b32 v61, v41, v40, 16\n"
"v_mfma_f32_16x16x32_bf16 v[212:215], a[84:87], a[52:55], v[212:215]\n"
"v_alignbit_b32 v108, v43, v42, 16\n"
"v_alignbit_b32 v109, v44, v43, 16\n"
"v_mfma_f32_16x16x32_bf16 v[216:219], a[84:87], a[56:59], v[216:219]\n"
"ds_read_b64_tr_b16 a[40:41], v20 offset:40960\n"
"v_alignbit_b32 v110, v45, v44, 16\n"
"v_mfma_f32_16x16x32_bf16 v[220:223], a[84:87], a[60:63], v[220:223]\n"
"ds_read_b64_tr_b16 a[42:43], v20 offset:41984\n"
"v_alignbit_b32 v111, v46, v45, 16\n"
"v_mfma_f32_16x16x32_bf16 v[224:227], a[88:91], a[48:51], v[224:227]\n"
"ds_read_b64_tr_b16 a[44:45], v21 offset:40960\n"
"v_mov_b32 v116, v43\n"
"v_mfma_f32_16x16x32_bf16 v[228:231], a[88:91], a[52:55], v[228:231]\n"
"v_mov_b32 v117, v44\n"
"v_mov_b32 v118, v45\n"
"v_mfma_f32_16x16x32_bf16 v[232:235], a[88:91], a[56:59], v[232:235]\n"
"ds_read_b64_tr_b16 a[46:47], v21 offset:41984\n"
"v_mov_b32 v119, v46\n"
"v_mfma_f32_16x16x32_bf16 v[236:239], a[88:91], a[60:63], v[236:239]\n"
"v_alignbit_b32 v112, v44, v43, 16\n"
"v_alignbit_b32 v113, v45, v44, 16\n"
"v_mfma_f32_16x16x32_bf16 v[240:243], a[92:95], a[48:51], v[240:243]\n"
"v_alignbit_b32 v114, v46, v45, 16\n"
"v_alignbit_b32 v115, v47, v46, 16\n"
"v_mfma_f32_16x16x32_bf16 v[244:247], a[92:95], a[52:55], v[244:247]\n"
"v_alignbit_b32 v53, v49, v48, 16\n"
"v_add_u32 v51, 0x20100, v125\n"
"v_mfma_f32_16x16x32_bf16 v[248:251], a[92:95], a[56:59], v[248:251]\n"
"s_lshl_b32 s29, s7, 3\n"
"v_and_b32 v62, 31, v0\n"
"v_mfma_f32_16x16x32_bf16 v[252:255], a[92:95], a[60:63], v[252:255]\n"
"s_waitcnt vmcnt(23) lgkmcnt(0)\n"
"s_barrier\n"
"v_mfma_f32_16x16x32_bf16 v[128:131], a[16:19], a[0:3], v[128:131]\n"
"ds_read_b64_tr_b16 a[64:65], v18 offset:49152\n"
"s_add_u32 m0, s26, 0x4100\n"
"s_add_u32 s24, s86, 0x10000\n"
"s_cmp_ge_u32 s24, 0x70000\n"
"s_cselect_b32 s31, 0x70000, 0\n"
"s_sub_u32 s24, s24, s31\n"
"v_mfma_f32_16x16x32_bf16 v[132:135], a[16:19], a[4:7], v[132:135]\n"
"buffer_load_dwordx4 v30, s[40:43], s24 offen offset:0 lds\n"
"s_add_u32 s28, s28, s29\n"
"v_mfma_f32_16x16x32_bf16 v[136:139], a[16:19], a[8:11], v[136:139]\n"
"ds_read_b64_tr_b16 a[66:67], v18 offset:50176\n"
"s_add_u32 s28, s28, 0xc000\n"
"v_mfma_f32_16x16x32_bf16 v[140:143], a[16:19], a[12:15], v[140:143]\n"
"ds_read_b128 a[48:51], v17 offset:32768\n"
"v_lshl_add_u32 v62, v62, 4, s87\n"
"v_mfma_f32_16x16x32_bf16 v[144:147], a[20:23], a[0:3], v[144:147]\n"
"ds_read_b128 a[52:55], v17 offset:34816\n"
"v_mov_b32 v63, s30\n"
"v_mfma_f32_16x16x32_bf16 v[148:151], a[20:23], a[4:7], v[148:151]\n"
"buffer_load_dwordx4 v31, s[40:43], s24 offen offset:1024 lds\n"
"v_mfma_f32_16x16x32_bf16 v[152:155], a[20:23], a[8:11], v[152:155]\n"
"ds_read_b128 a[56:59], v17 offset:36864\n"
"v_mfma_f32_16x16x32_bf16 v[156:159], a[20:23], a[12:15], v[156:159]\n"
"ds_read_b128 a[60:63], v17 offset:38912\n"
"v_mfma_f32_16x16x32_bf16 v[160:163], a[24:27], a[0:3], v[160:163]\n"
"ds_read_b64_tr_b16 a[68:69], v19 offset:49152\n"
"v_mfma_f32_16x16x32_bf16 v[164:167], a[24:27], a[4:7], v[164:167]\n"
"buffer_load_dwordx4 v32, s[40:43], s24 offen offset:2048 lds\n"
"v_mfma_f32_16x16x32_bf16 v[168:171], a[24:27], a[8:11], v[168:171]\n"
"ds_read_b64_tr_b16 a[70:71], v19 offset:50176\n"
"v_mfma_f32_16x16x32_bf16 v[172:175], a[24:27], a[12:15], v[172:175]\n"
"ds_read_b64_tr_b16 a[72:73], v20 offset:49152\n"
"v_mfma_f32_16x16x32_bf16 v[176:179], a[28:31], a[0:3], v[176:179]\n"
"ds_read_b64_tr_b16 a[74:75], v20 offset:50176\n"
"v_mfma_f32_16x16x32_bf16 v[180:183], a[28:31], a[4:7], v[180:183]\n"
"buffer_load_dwordx4 v33, s[40:43], s24 offen offset:3072 lds\n"
"v_mfma_f32_16x16x32_bf16 v[184:187], a[28:31], a[8:11], v[184:187]\n"
"ds_read_b64_tr_b16 a[76:77], v21 offset:49152\n"
"v_mfma_f32_16x16x32_bf16 v[188:191], a[28:31], a[12:15], v[188:191]\n"
"ds_read_b64_tr_b16 a[78:79], v21 offset:50176\n"
"v_mfma_f32_16x16x32_bf16 v[192:195], a[32:35], a[0:3], v[192:195]\n"
"ds_read_b64_tr_b16 a[80:81], v18 offset:57344\n"
"s_lshl_b32 s27, s7, 10\n"
"v_mfma_f32_16x16x32_bf16 v[196:199], a[32:35], a[4:7], v[196:199]\n"
"s_add_u32 s27, s27, 159744\n"
"v_add_u32 v7, s27, v7\n"
"v_mfma_f32_16x16x32_bf16 v[200:203], a[32:35], a[8:11], v[200:203]\n"
"ds_read_b64_tr_b16 a[82:83], v18 offset:58368\n"
"s_waitcnt vmcnt(10)\n"
"v_mfma_f32_16x16x32_bf16 v[204:207], a[32:35], a[12:15], v[204:207]\n"
"ds_read_b64_tr_b16 a[84:85], v19 offset:57344\n"
"ds_write_b64 v7, v[120:121]\n"
"v_mfma_f32_16x16x32_bf16 v[208:211], a[36:39], a[0:3], v[208:211]\n"
"ds_read_b64_tr_b16 a[86:87], v19 offset:58368\n"
"ds_write_b64 v7, v[122:123] offset:512\n"
"v_mfma_f32_16x16x32_bf16 v[212:215], a[36:39], a[4:7], v[212:215]\n"
"v_lshl_add_u32 v6, v1, 4, s27\n"
"v_lshl_add_u32 v7, v2, 3, s27\n"
"v_mfma_f32_16x16x32_bf16 v[216:219], a[36:39], a[8:11], v[216:219]\n"
"ds_read_b64_tr_b16 a[88:89], v20 offset:57344\n"
"v_mfma_f32_16x16x32_bf16 v[220:223], a[36:39], a[12:15], v[220:223]\n"
"ds_read_b64_tr_b16 a[90:91], v20 offset:58368\n"
"v_mfma_f32_16x16x32_bf16 v[224:227], a[40:43], a[0:3], v[224:227]\n"
"ds_read_b64_tr_b16 a[92:93], v21 offset:57344\n"
"v_mfma_f32_16x16x32_bf16 v[228:231], a[40:43], a[4:7], v[228:231]\n"
"v_mfma_f32_16x16x32_bf16 v[232:235], a[40:43], a[8:11], v[232:235]\n"
"ds_read_b64_tr_b16 a[94:95], v21 offset:58368\n"
"v_mfma_f32_16x16x32_bf16 v[236:239], a[40:43], a[12:15], v[236:239]\n"
"v_mfma_f32_16x16x32_bf16 v[240:243], a[44:47], a[0:3], v[240:243]\n"
"v_mfma_f32_16x16x32_bf16 v[244:247], a[44:47], a[4:7], v[244:247]\n"
"s_waitcnt lgkmcnt(4)\n"
"ds_read_b64 v[96:97], v7 offset:512\n"
"v_mfma_f32_16x16x32_bf16 v[248:251], a[44:47], a[8:11], v[248:251]\n"
"ds_read_b64 v[98:99], v7 offset:640\n"
"ds_read_b64 v[100:101], v7 offset:768\n"
"v_mfma_f32_16x16x32_bf16 v[252:255], a[44:47], a[12:15], v[252:255]\n"
"s_waitcnt vmcnt(18) lgkmcnt(0)\n"
"s_barrier\n"
"v_mfma_f32_16x16x32_bf16 v[128:131], a[64:67], a[48:51], v[128:131]\n"
"ds_read_b64_tr_b16 a[16:17], v22 offset:0\n"
"s_add_u32 m0, s26, 0x8100\n"
"s_add_u32 s24, s86, 0x20000\n"
"s_cmp_ge_u32 s24, 0x70000\n"
"s_cselect_b32 s31, 0x70000, 0\n"
"s_sub_u32 s24, s24, s31\n"
"v_mfma_f32_16x16x32_bf16 v[132:135], a[64:67], a[52:55], v[132:135]\n"
"buffer_load_dwordx4 v30, s[40:43], s24 offen offset:0 lds\n"
"ds_read_b64 v[102:103], v7 offset:896\n"
"v_mfma_f32_16x16x32_bf16 v[136:139], a[64:67], a[56:59], v[136:139]\n"
"ds_read_b64_tr_b16 a[18:19], v22 offset:1024\n"
"ds_read_b128 v[64:67], v6 offset:0\n"
"v_mfma_f32_16x16x32_bf16 v[140:143], a[64:67], a[60:63], v[140:143]\n"
"ds_read_b128 a[0:3], v16 offset:49152\n"
"ds_read_b128 v[68:71], v6 offset:64\n"
"v_mfma_f32_16x16x32_bf16 v[144:147], a[68:71], a[48:51], v[144:147]\n"
"ds_read_b128 a[4:7], v16 offset:51200\n"
"ds_read_b128 v[72:75], v6 offset:128\n"
"v_mfma_f32_16x16x32_bf16 v[148:151], a[68:71], a[52:55], v[148:151]\n"
"buffer_load_dwordx4 v31, s[40:43], s24 offen offset:1024 lds\n"
"ds_read_b128 v[76:79], v6 offset:192\n"
"v_mfma_f32_16x16x32_bf16 v[152:155], a[68:71], a[56:59], v[152:155]\n"
"ds_read_b128 a[8:11], v16 offset:53248\n"
"ds_read_b128 v[80:83], v6 offset:256\n"
"v_mfma_f32_16x16x32_bf16 v[156:159], a[68:71], a[60:63], v[156:159]\n"
"ds_read_b128 a[12:15], v16 offset:55296\n"
"ds_read_b128 v[84:87], v6 offset:320\n"
"v_mfma_f32_16x16x32_bf16 v[160:163], a[72:75], a[48:51], v[160:163]\n"
"ds_read_b64_tr_b16 a[20:21], v23 offset:0\n"
"ds_read_b128 v[88:91], v6 offset:384\n"
"v_mfma_f32_16x16x32_bf16 v[164:167], a[72:75], a[52:55], v[164:167]\n"
"buffer_load_dwordx4 v32, s[40:43], s24 offen offset:2048 lds\n"
"ds_read_b128 v[92:95], v6 offset:448\n"
"v_mfma_f32_16x16x32_bf16 v[168:171], a[72:75], a[56:59], v[168:171]\n"
"ds_read_b64_tr_b16 a[22:23], v23 offset:1024\n"
"v_mfma_f32_16x16x32_bf16 v[172:175], a[72:75], a[60:63], v[172:175]\n"
"ds_read_b64_tr_b16 a[24:25], v24 offset:0\n"
"v_mfma_f32_16x16x32_bf16 v[176:179], a[76:79], a[48:51], v[176:179]\n"
"ds_read_b64_tr_b16 a[26:27], v24 offset:1024\n"
"v_mfma_f32_16x16x32_bf16 v[180:183], a[76:79], a[52:55], v[180:183]\n"
"buffer_load_dwordx4 v33, s[40:43], s24 offen offset:3072 lds\n"
"v_mfma_f32_16x16x32_bf16 v[184:187], a[76:79], a[56:59], v[184:187]\n"
"ds_read_b64_tr_b16 a[28:29], v25 offset:0\n"
"v_mfma_f32_16x16x32_bf16 v[188:191], a[76:79], a[60:63], v[188:191]\n"
"ds_read_b64_tr_b16 a[30:31], v25 offset:1024\n"
"s_add_u32 m0, s26, 0x18100\n"
"v_mfma_f32_16x16x32_bf16 v[192:195], a[80:83], a[48:51], v[192:195]\n"
"ds_read_b64_tr_b16 a[32:33], v22 offset:8192\n"
"s_add_u32 s24, s86, 0x30000\n"
"s_cmp_ge_u32 s24, 0x70000\n"
"s_cselect_b32 s31, 0x70000, 0\n"
"s_sub_u32 s24, s24, s31\n"
"v_mfma_f32_16x16x32_bf16 v[196:199], a[80:83], a[52:55], v[196:199]\n"
"buffer_load_dwordx4 v30, s[40:43], s24 offen offset:0 lds\n"
"v_mfma_f32_16x16x32_bf16 v[200:203], a[80:83], a[56:59], v[200:203]\n"
"ds_read_b64_tr_b16 a[34:35], v22 offset:9216\n"
"v_mfma_f32_16x16x32_bf16 v[204:207], a[80:83], a[60:63], v[204:207]\n"
"ds_read_b64_tr_b16 a[36:37], v23 offset:8192\n"
"v_mfma_f32_16x16x32_bf16 v[208:211], a[84:87], a[48:51], v[208:211]\n"
"ds_read_b64_tr_b16 a[38:39], v23 offset:9216\n"
"v_mfma_f32_16x16x32_bf16 v[212:215], a[84:87], a[52:55], v[212:215]\n"
"buffer_load_dwordx4 v31, s[40:43], s24 offen offset:1024 lds\n"
"v_mfma_f32_16x16x32_bf16 v[216:219], a[84:87], a[56:59], v[216:219]\n"
"ds_read_b64_tr_b16 a[40:41], v24 offset:8192\n"
"v_mfma_f32_16x16x32_bf16 v[220:223], a[84:87], a[60:63], v[220:223]\n"
"ds_read_b64_tr_b16 a[42:43], v24 offset:9216\n"
"v_mfma_f32_16x16x32_bf16 v[224:227], a[88:91], a[48:51], v[224:227]\n"
"ds_read_b64_tr_b16 a[44:45], v25 offset:8192\n"
"v_mfma_f32_16x16x32_bf16 v[228:231], a[88:91], a[52:55], v[228:231]\n"
"buffer_load_dwordx4 v32, s[40:43], s24 offen offset:2048 lds\n"
"v_mfma_f32_16x16x32_bf16 v[232:235], a[88:91], a[56:59], v[232:235]\n"
"ds_read_b64_tr_b16 a[46:47], v25 offset:9216\n"
"v_mfma_f32_16x16x32_bf16 v[236:239], a[88:91], a[60:63], v[236:239]\n"
"v_mfma_f32_16x16x32_bf16 v[240:243], a[92:95], a[48:51], v[240:243]\n"
"v_mfma_f32_16x16x32_bf16 v[244:247], a[92:95], a[52:55], v[244:247]\n"
"buffer_load_dwordx4 v33, s[40:43], s24 offen offset:3072 lds\n"
"v_mfma_f32_16x16x32_bf16 v[248:251], a[92:95], a[56:59], v[248:251]\n"
"v_mfma_f32_16x16x32_bf16 v[252:255], a[92:95], a[60:63], v[252:255]\n"
"s_waitcnt vmcnt(20) lgkmcnt(0)\n"
"s_barrier\n"
"v_mfma_f32_16x16x32_bf16 v[128:131], a[16:19], a[0:3], v[128:131]\n"
"ds_read_b64_tr_b16 a[64:65], v18 offset:0\n"
"s_add_u32 m0, s26, 0xc100\n"
"s_add_u32 s24, s86, 0x40000\n"
"s_cmp_ge_u32 s24, 0x70000\n"
"s_cselect_b32 s31, 0x70000, 0\n"
"s_sub_u32 s24, s24, s31\n"
"v_mfma_f32_16x16x32_bf16 v[132:135], a[16:19], a[4:7], v[132:135]\n"
"buffer_load_dwordx4 v30, s[40:43], s24 offen offset:0 lds\n"
"v_sub_u32 v28, 15, v2\n"
"v_mfma_f32_16x16x32_bf16 v[136:139], a[16:19], a[8:11], v[136:139]\n"
"ds_read_b64_tr_b16 a[66:67], v18 offset:1024\n"
"v_lshlrev_b32 v5, 3, v1\n"
"v_mfma_f32_16x16x32_bf16 v[140:143], a[16:19], a[12:15], v[140:143]\n"
"ds_read_b128 a[48:51], v17 offset:49152\n"
"v_sub_u32 v28, v28, v5\n"
"v_mfma_f32_16x16x32_bf16 v[144:147], a[20:23], a[0:3], v[144:147]\n"
"ds_read_b128 a[52:55], v17 offset:51200\n"
"s_and_b32 s22, s7, 1\n"
"v_mfma_f32_16x16x32_bf16 v[148:151], a[20:23], a[4:7], v[148:151]\n"
"buffer_load_dwordx4 v31, s[40:43], s24 offen offset:1024 lds\n"
"s_lshl_b32 s22, s22, 6\n"
"v_mfma_f32_16x16x32_bf16 v[152:155], a[20:23], a[8:11], v[152:155]\n"
"ds_read_b128 a[56:59], v17 offset:53248\n"
"v_or_b32 v28, s22, v28\n"
"v_mfma_f32_16x16x32_bf16 v[156:159], a[20:23], a[12:15], v[156:159]\n"
"ds_read_b128 a[60:63], v17 offset:55296\n"
"v_lshlrev_b32 v5, 16, v50\n"
"v_mfma_f32_16x16x32_bf16 v[160:163], a[24:27], a[0:3], v[160:163]\n"
"ds_read_b64_tr_b16 a[68:69], v19 offset:0\n"
"v_cmp_eq_u32 s[58:59], 0, v28\n"
"v_mfma_f32_16x16x32_bf16 v[164:167], a[24:27], a[4:7], v[164:167]\n"
"buffer_load_dwordx4 v32, s[40:43], s24 offen offset:2048 lds\n"
"v_cmp_eq_u32 s[56:57], 1, v28\n"
"v_mfma_f32_16x16x32_bf16 v[168:171], a[24:27], a[8:11], v[168:171]\n"
"ds_read_b64_tr_b16 a[70:71], v19 offset:1024\n"
"v_accvgpr_write_b32 a208, 0\n"
"v_mfma_f32_16x16x32_bf16 v[172:175], a[24:27], a[12:15], v[172:175]\n"
"ds_read_b64_tr_b16 a[72:73], v20 offset:0\n"
"v_cndmask_b32 v24, 0, v50, s[58:59]\n"
"v_mfma_f32_16x16x32_bf16 v[176:179], a[28:31], a[0:3], v[176:179]\n"
"ds_read_b64_tr_b16 a[74:75], v20 offset:1024\n"
"s_nop 0\n"
"v_mfma_f32_16x16x32_bf16 v[180:183], a[28:31], a[4:7], v[180:183]\n"
"buffer_load_dwordx4 v33, s[40:43], s24 offen offset:3072 lds\n"
"v_cndmask_b32 v24, v24, v5, s[56:57]\n"
"v_mfma_f32_16x16x32_bf16 v[184:187], a[28:31], a[8:11], v[184:187]\n"
"ds_read_b64_tr_b16 a[76:77], v21 offset:0\n"
"v_cmp_eq_u32 s[58:59], 2, v28\n"
"v_mfma_f32_16x16x32_bf16 v[188:191], a[28:31], a[12:15], v[188:191]\n"
"ds_read_b64_tr_b16 a[78:79], v21 offset:1024\n"
"s_add_u32 m0, s26, 0x10100\n"
"v_mfma_f32_16x16x32_bf16 v[192:195], a[32:35], a[0:3], v[192:195]\n"
"ds_read_b64_tr_b16 a[80:81], v18 offset:8192\n"
"s_add_u32 s24, s86, 0x50000\n"
"s_cmp_ge_u32 s24, 0x70000\n"
"s_cselect_b32 s31, 0x70000, 0\n"
"s_sub_u32 s24, s24, s31\n"
"v_mfma_f32_16x16x32_bf16 v[196:199], a[32:35], a[4:7], v[196:199]\n"
"buffer_load_dwordx4 v30, s[40:43], s24 offen offset:0 lds\n"
"v_cmp_eq_u32 s[56:57], 3, v28\n"
"v_mfma_f32_16x16x32_bf16 v[200:203], a[32:35], a[8:11], v[200:203]\n"
"ds_read_b64_tr_b16 a[82:83], v18 offset:9216\n"
"v_accvgpr_write_b32 a209, 0\n"
"v_mfma_f32_16x16x32_bf16 v[204:207], a[32:35], a[12:15], v[204:207]\n"
"ds_read_b64_tr_b16 a[84:85], v19 offset:8192\n"
"v_cndmask_b32 v25, 0, v50, s[58:59]\n"
"v_mfma_f32_16x16x32_bf16 v[208:211], a[36:39], a[0:3], v[208:211]\n"
"ds_read_b64_tr_b16 a[86:87], v19 offset:9216\n"
"s_nop 0\n"
"v_mfma_f32_16x16x32_bf16 v[212:215], a[36:39], a[4:7], v[212:215]\n"
"buffer_load_dwordx4 v31, s[40:43], s24 offen offset:1024 lds\n"
"v_cndmask_b32 v25, v25, v5, s[56:57]\n"
"v_mfma_f32_16x16x32_bf16 v[216:219], a[36:39], a[8:11], v[216:219]\n"
"ds_read_b64_tr_b16 a[88:89], v20 offset:8192\n"
"v_cmp_eq_u32 s[58:59], 4, v28\n"
"v_mfma_f32_16x16x32_bf16 v[220:223], a[36:39], a[12:15], v[220:223]\n"
"ds_read_b64_tr_b16 a[90:91], v20 offset:9216\n"
"v_cmp_eq_u32 s[56:57], 5, v28\n"
"v_mfma_f32_16x16x32_bf16 v[224:227], a[40:43], a[0:3], v[224:227]\n"
"ds_read_b64_tr_b16 a[92:93], v21 offset:8192\n"
"v_accvgpr_write_b32 a210, 0\n"
"v_mfma_f32_16x16x32_bf16 v[228:231], a[40:43], a[4:7], v[228:231]\n"
"buffer_load_dwordx4 v32, s[40:43], s24 offen offset:2048 lds\n"
"v_cndmask_b32 v26, 0, v50, s[58:59]\n"
"v_mfma_f32_16x16x32_bf16 v[232:235], a[40:43], a[8:11], v[232:235]\n"
"ds_read_b64_tr_b16 a[94:95], v21 offset:9216\n"
"s_nop 0\n"
"v_cndmask_b32 v26, v26, v5, s[56:57]\n"
"v_mfma_f32_16x16x32_bf16 v[236:239], a[40:43], a[12:15], v[236:239]\n"
"v_cmp_eq_u32 s[58:59], 6, v28\n"
"v_cmp_eq_u32 s[56:57], 7, v28\n"
"v_mfma_f32_16x16x32_bf16 v[240:243], a[44:47], a[0:3], v[240:243]\n"
"v_accvgpr_write_b32 a211, 0\n"
"v_cndmask_b32 v27, 0, v50, s[58:59]\n"
"v_mfma_f32_16x16x32_bf16 v[244:247], a[44:47], a[4:7], v[244:247]\n"
"s_waitcnt vmcnt(23)\n"
"v_mov_b32 v120, s34\n"
"v_mov_b32 v121, s35\n"
"v_mov_b32 v122, s28\n"
"s_mov_b64 exec, 1\n"
"buffer_store_dwordx2 v[120:121], v122, s[16:19], 0 offen sc1\n"
"s_mov_b64 exec, -1\n"
"v_mfma_f32_16x16x32_bf16 v[248:251], a[44:47], a[8:11], v[248:251]\n"
"s_nop 0\n"
"v_cndmask_b32 v27, v27, v5, s[56:57]\n"
"v_mfma_f32_16x16x32_bf16 v[252:255], a[44:47], a[12:15], v[252:255]\n"
"s_waitcnt lgkmcnt(0)\n"
"s_barrier\n"
"v_mfma_f32_16x16x32_bf16 v[128:131], a[64:67], a[48:51], v[128:131]\n"
"ds_write_b128 v51, v[108:111] offset:0\n"
"s_waitcnt vmcnt(16)\n"
"v_mfma_f32_16x16x32_bf16 v[132:135], a[64:67], a[52:55], v[132:135]\n"
"buffer_load_dwordx4 v33, s[40:43], s24 offen offset:3072 lds\n"
"ds_write_b128 v51, v[116:119] offset:4160\n"
"v_mfma_f32_16x16x32_bf16 v[136:139], a[64:67], a[56:59], v[136:139]\n"
"ds_write_b128 v51, v[112:115] offset:8320\n"
"ds_read_b64_tr_b16 a[96:97], v12 offset:16640\n"
"v_mfma_f32_16x16x32_bf16 v[140:143], a[64:67], a[60:63], v[140:143]\n"
"s_add_u32 m0, s26, 0x1c100\n"
"ds_write_b128 v51, v[44:47] offset:12480\n"
"v_mfma_f32_16x16x32_bf16 v[144:147], a[68:71], a[48:51], v[144:147]\n"
"s_add_u32 s24, s86, 0x60000\n"
"s_cmp_ge_u32 s24, 0x70000\n"
"s_cselect_b32 s31, 0x70000, 0\n"
"s_sub_u32 s24, s24, s31\n"
"v_mfma_f32_16x16x32_bf16 v[148:151], a[68:71], a[52:55], v[148:151]\n"
"buffer_load_dwordx4 v30, s[40:43], s24 offen offset:0 lds\n"
"ds_read_b64_tr_b16 a[98:99], v12 offset:18688\n"
"v_mfma_f32_16x16x32_bf16 v[152:155], a[68:71], a[56:59], v[152:155]\n"
"ds_read_b64_tr_b16 a[100:101], v13 offset:16640\n"
"ds_write_b128 v125, v[54:57] offset:0\n"
"v_mfma_f32_16x16x32_bf16 v[156:159], a[68:71], a[60:63], v[156:159]\n"
"ds_read_b64_tr_b16 a[102:103], v13 offset:18688\n"
"ds_read_b64_tr_b16 a[104:105], v14 offset:16640\n"
"v_mfma_f32_16x16x32_bf16 v[160:163], a[72:75], a[48:51], v[160:163]\n"
"ds_read_b64_tr_b16 a[106:107], v14 offset:18688\n"
"ds_write_b128 v125, v[104:107] offset:4160\n"
"v_mfma_f32_16x16x32_bf16 v[164:167], a[72:75], a[52:55], v[164:167]\n"
"buffer_load_dwordx4 v31, s[40:43], s24 offen offset:1024 lds\n"
"ds_read_b64_tr_b16 a[108:109], v15 offset:16640\n"
"v_mfma_f32_16x16x32_bf16 v[168:171], a[72:75], a[56:59], v[168:171]\n"
"ds_read_b64_tr_b16 a[110:111], v15 offset:18688\n"
"ds_write_b128 v125, v[58:61] offset:8320\n"
"v_mfma_f32_16x16x32_bf16 v[172:175], a[72:75], a[60:63], v[172:175]\n"
"ds_read_b64_tr_b16 a[112:113], v8 offset:16640\n"
"ds_read_b64_tr_b16 a[114:115], v8 offset:18688\n"
"v_mfma_f32_16x16x32_bf16 v[176:179], a[76:79], a[48:51], v[176:179]\n"
"ds_write_b128 v125, v[38:41] offset:12480\n"
"ds_read_b64_tr_b16 a[116:117], v9 offset:16640\n"
"v_mfma_f32_16x16x32_bf16 v[180:183], a[76:79], a[52:55], v[180:183]\n"
"buffer_load_dwordx4 v32, s[40:43], s24 offen offset:2048 lds\n"
"ds_read_b64_tr_b16 a[118:119], v9 offset:18688\n"
"v_mfma_f32_16x16x32_bf16 v[184:187], a[76:79], a[56:59], v[184:187]\n"
"ds_write_b128 v62, a[208:211] offset:4096\n"
"ds_read_b64_tr_b16 a[120:121], v10 offset:16640\n"
"v_mfma_f32_16x16x32_bf16 v[188:191], a[76:79], a[60:63], v[188:191]\n"
"ds_read_b64_tr_b16 a[122:123], v10 offset:18688\n"
"s_mov_b64 exec, 0xff\n"
"ds_write_b128 v124, a[208:211]\n"
"s_mov_b64 exec, 1\n"
"ds_write_b16 v63, v48 offset:30\n"
"ds_write_b32 v63, v48 offset:4188\n"
"ds_write_b16 v63, v48 offset:8346\n"
"ds_write_b32 v63, v53 offset:8348\n"
"ds_write_b64 v63, v[48:49] offset:12504\n"
"s_mov_b64 exec, -1\n"
"v_mfma_f32_16x16x32_bf16 v[192:195], a[80:83], a[48:51], v[192:195]\n"
"ds_read_b64_tr_b16 a[124:125], v11 offset:16640\n"
"ds_read_b64_tr_b16 a[126:127], v11 offset:18688\n"
"v_mfma_f32_16x16x32_bf16 v[196:199], a[80:83], a[52:55], v[196:199]\n"
"buffer_load_dwordx4 v33, s[40:43], s24 offen offset:3072 lds\n"
"v_mfma_f32_16x16x32_bf16 v[200:203], a[80:83], a[56:59], v[200:203]\n"
"s_add_u32 m0, s26, 0x14100\n"
"s_add_u32 s24, s86, 0x8000\n"
"s_cmp_ge_u32 s24, 0x70000\n"
"s_cselect_b32 s31, 0x70000, 0\n"
"s_sub_u32 s24, s24, s31\n"
"v_mfma_f32_16x16x32_bf16 v[204:207], a[80:83], a[60:63], v[204:207]\n"
"s_waitcnt lgkmcnt(15)\n"
"v_mul_f32 v3, v97, v97\n"
"v_mul_f32 v104, 0xc0135761, v97\n"
"v_rcp_f32 v106, v3\n"
"v_mul_f32 v3, v3, v104\n"
"v_mul_f32 v105, 0x3d372713, v3\n"
"v_mul_f32 v3, v99, v99\n"
"v_mul_f32 v108, 0xc0135761, v99\n"
"v_rcp_f32 v110, v3\n"
"v_mfma_f32_16x16x32_bf16 v[208:211], a[84:87], a[48:51], v[208:211]\n"
"v_mul_f32 v3, v3, v108\n"
"v_mul_f32 v109, 0x3d372713, v3\n"
"v_mfma_f32_16x16x32_bf16 v[212:215], a[84:87], a[52:55], v[212:215]\n"
"v_mul_f32 v3, v101, v101\n"
"buffer_load_dwordx4 v30, s[40:43], s24 offen offset:0 lds\n"
"v_mfma_f32_16x16x32_bf16 v[216:219], a[84:87], a[56:59], v[216:219]\n"
"v_mul_f32 v112, 0xc0135761, v101\n"
"v_rcp_f32 v114, v3\n"
"v_mfma_f32_16x16x32_bf16 v[220:223], a[84:87], a[60:63], v[220:223]\n"
"v_mul_f32 v3, v3, v112\n"
"v_mul_f32 v113, 0x3d372713, v3\n"
"v_mfma_f32_16x16x32_bf16 v[224:227], a[88:91], a[48:51], v[224:227]\n"
"v_mul_f32 v3, v103, v103\n"
"v_mul_f32 v116, 0xc0135761, v103\n"
"v_mfma_f32_16x16x32_bf16 v[228:231], a[88:91], a[52:55], v[228:231]\n"
"buffer_load_dwordx4 v31, s[40:43], s24 offen offset:1024 lds\n"
"v_rcp_f32 v118, v3\n"
"v_mfma_f32_16x16x32_bf16 v[232:235], a[88:91], a[56:59], v[232:235]\n"
"v_mul_f32 v3, v3, v116\n"
"v_mul_f32 v117, 0x3d372713, v3\n"
"v_mfma_f32_16x16x32_bf16 v[236:239], a[88:91], a[60:63], v[236:239]\n"
"v_mfma_f32_16x16x32_bf16 v[240:243], a[92:95], a[48:51], v[240:243]\n"
"v_mfma_f32_16x16x32_bf16 v[244:247], a[92:95], a[52:55], v[244:247]\n"
"buffer_load_dwordx4 v32, s[40:43], s24 offen offset:2048 lds\n"
"v_mfma_f32_16x16x32_bf16 v[248:251], a[92:95], a[56:59], v[248:251]\n"
"v_mfma_f32_16x16x32_bf16 v[252:255], a[92:95], a[60:63], v[252:255]\n"
"s_waitcnt lgkmcnt(0)\n"
"v_pk_fma_f32 v[128:129], v[64:65], v[96:97], v[128:129] op_sel_hi:[1,0,1] neg_lo:[0,1,0] neg_hi:[0,1,0]\n"
"v_pk_fma_f32 v[130:131], v[66:67], v[96:97], v[130:131] op_sel_hi:[1,0,1] neg_lo:[0,1,0] neg_hi:[0,1,0]\n"
"v_pk_fma_f32 v[144:145], v[68:69], v[96:97], v[144:145] op_sel_hi:[1,0,1] neg_lo:[0,1,0] neg_hi:[0,1,0]\n"
"v_pk_fma_f32 v[146:147], v[70:71], v[96:97], v[146:147] op_sel_hi:[1,0,1] neg_lo:[0,1,0] neg_hi:[0,1,0]\n"
"v_pk_mul_f32 v[16:17], v[128:129], v[128:129]\n"
"v_pk_mul_f32 v[18:19], v[130:131], v[130:131]\n"
"v_pk_mul_f32 v[20:21], v[144:145], v[144:145]\n"
"v_pk_mul_f32 v[22:23], v[146:147], v[146:147]\n"
"v_pk_fma_f32 v[16:17], v[104:105], v[16:17], v[104:105] op_sel:[1,0,0] op_sel_hi:[1,1,0]\n"
"v_pk_fma_f32 v[18:19], v[104:105], v[18:19], v[104:105] op_sel:[1,0,0] op_sel_hi:[1,1,0]\n"
"v_pk_fma_f32 v[20:21], v[104:105], v[20:21], v[104:105] op_sel:[1,0,0] op_sel_hi:[1,1,0]\n"
"buffer_load_dwordx2 v[62:63], v126, s[16:19], 0 offen sc1\n"
"v_pk_fma_f32 v[22:23], v[104:105], v[22:23], v[104:105] op_sel:[1,0,0] op_sel_hi:[1,1,0]\n"
"v_pk_mul_f32 v[16:17], v[128:129], v[16:17]\n"
"v_pk_mul_f32 v[18:19], v[130:131], v[18:19]\n"
"v_pk_mul_f32 v[20:21], v[144:145], v[20:21]\n"
"v_pk_mul_f32 v[22:23], v[146:147], v[22:23]\n"
"v_exp_f32 v16, v16\n"
"v_pk_fma_f32 v[192:193], v[80:81], v[96:97], v[192:193] op_sel_hi:[1,0,1] neg_lo:[0,1,0] neg_hi:[0,1,0]\n"
"v_exp_f32 v17, v17\n"
"v_pk_fma_f32 v[194:195], v[82:83], v[96:97], v[194:195] op_sel_hi:[1,0,1] neg_lo:[0,1,0] neg_hi:[0,1,0]\n"
"v_exp_f32 v18, v18\n"
"v_pk_fma_f32 v[208:209], v[84:85], v[96:97], v[208:209] op_sel_hi:[1,0,1] neg_lo:[0,1,0] neg_hi:[0,1,0]\n"
"v_exp_f32 v19, v19\n"
"v_pk_fma_f32 v[210:211], v[86:87], v[96:97], v[210:211] op_sel_hi:[1,0,1] neg_lo:[0,1,0] neg_hi:[0,1,0]\n"
"v_exp_f32 v20, v20\n"
"v_pk_mul_f32 v[192:193], v[128:129], v[192:193]\n"
"v_exp_f32 v21, v21\n"
"v_pk_mul_f32 v[194:195], v[130:131], v[194:195]\n"
"v_exp_f32 v22, v22\n"
"v_pk_mul_f32 v[208:209], v[144:145], v[208:209]\n"
"v_exp_f32 v23, v23\n"
"v_pk_mul_f32 v[210:211], v[146:147], v[210:211]\n"
"v_pk_fma_f32 v[16:17], v[16:17], v[106:107], v[106:107] op_sel_hi:[1,0,0]\n"
"v_pk_fma_f32 v[18:19], v[18:19], v[106:107], v[106:107] op_sel_hi:[1,0,0]\n"
"v_pk_fma_f32 v[20:21], v[20:21], v[106:107], v[106:107] op_sel_hi:[1,0,0]\n"
"v_pk_fma_f32 v[22:23], v[22:23], v[106:107], v[106:107] op_sel_hi:[1,0,0]\n"
"v_rcp_f32 v16, v16\n"
"v_pk_fma_f32 v[132:133], v[64:65], v[98:99], v[132:133] op_sel_hi:[1,0,1] neg_lo:[0,1,0] neg_hi:[0,1,0]\n"
"v_rcp_f32 v17, v17\n"
"v_pk_fma_f32 v[134:135], v[66:67], v[98:99], v[134:135] op_sel_hi:[1,0,1] neg_lo:[0,1,0] neg_hi:[0,1,0]\n"
"v_rcp_f32 v18, v18\n"
"v_pk_fma_f32 v[148:149], v[68:69], v[98:99], v[148:149] op_sel_hi:[1,0,1] neg_lo:[0,1,0] neg_hi:[0,1,0]\n"
"v_rcp_f32 v19, v19\n"
"v_pk_fma_f32 v[150:151], v[70:71], v[98:99], v[150:151] op_sel_hi:[1,0,1] neg_lo:[0,1,0] neg_hi:[0,1,0]\n"
"v_rcp_f32 v20, v20\n"
"v_pk_mul_f32 v[54:55], v[132:133], v[132:133]\n"
"v_rcp_f32 v21, v21\n"
"v_pk_mul_f32 v[56:57], v[134:135], v[134:135]\n"
"v_rcp_f32 v22, v22\n"
"v_pk_mul_f32 v[58:59], v[148:149], v[148:149]\n"
"v_rcp_f32 v23, v23\n"
"v_pk_mul_f32 v[60:61], v[150:151], v[150:151]\n"
"v_pk_mul_f32 v[192:193], v[192:193], v[16:17]\n"
"v_pk_mul_f32 v[194:195], v[194:195], v[18:19]\n"
"v_pk_mul_f32 v[208:209], v[208:209], v[20:21]\n"
"v_pk_mul_f32 v[210:211], v[210:211], v[22:23]\n"
"v_pk_fma_f32 v[54:55], v[108:109], v[54:55], v[108:109] op_sel:[1,0,0] op_sel_hi:[1,1,0]\n"
"v_pk_fma_f32 v[56:57], v[108:109], v[56:57], v[108:109] op_sel:[1,0,0] op_sel_hi:[1,1,0]\n"
"v_pk_fma_f32 v[58:59], v[108:109], v[58:59], v[108:109] op_sel:[1,0,0] op_sel_hi:[1,1,0]\n"
"v_pk_fma_f32 v[60:61], v[108:109], v[60:61], v[108:109] op_sel:[1,0,0] op_sel_hi:[1,1,0]\n"
"v_cvt_pk_bf16_f32 v4, v192, v193\n"
"v_cvt_pk_bf16_f32 v5, v194, v195\n"
"v_cvt_pk_bf16_f32 v6, v208, v209\n"
"v_cvt_pk_bf16_f32 v7, v210, v211\n"
"buffer_store_dwordx4 v[4:7], v52, s[88:91], s52 offen offset:0 sc1\n"
"v_pk_mul_f32 v[54:55], v[132:133], v[54:55]\n"
"v_pk_mul_f32 v[56:57], v[134:135], v[56:57]\n"
"v_pk_mul_f32 v[58:59], v[148:149], v[58:59]\n"
"v_pk_mul_f32 v[60:61], v[150:151], v[60:61]\n"
"v_exp_f32 v54, v54\n"
"v_pk_fma_f32 v[196:197], v[80:81], v[98:99], v[196:197] op_sel_hi:[1,0,1] neg_lo:[0,1,0] neg_hi:[0,1,0]\n"
"v_exp_f32 v55, v55\n"
"v_pk_fma_f32 v[198:199], v[82:83], v[98:99], v[198:199] op_sel_hi:[1,0,1] neg_lo:[0,1,0] neg_hi:[0,1,0]\n"
"v_exp_f32 v56, v56\n"
"v_pk_fma_f32 v[212:213], v[84:85], v[98:99], v[212:213] op_sel_hi:[1,0,1] neg_lo:[0,1,0] neg_hi:[0,1,0]\n"
"v_exp_f32 v57, v57\n"
"v_pk_fma_f32 v[214:215], v[86:87], v[98:99], v[214:215] op_sel_hi:[1,0,1] neg_lo:[0,1,0] neg_hi:[0,1,0]\n"
"v_exp_f32 v58, v58\n"
"v_pk_mul_f32 v[196:197], v[132:133], v[196:197]\n"
"v_exp_f32 v59, v59\n"
"v_pk_mul_f32 v[198:199], v[134:135], v[198:199]\n"
"v_exp_f32 v60, v60\n"
"v_pk_mul_f32 v[212:213], v[148:149], v[212:213]\n"
"v_exp_f32 v61, v61\n"
"v_pk_mul_f32 v[214:215], v[150:151], v[214:215]\n"
"v_pk_fma_f32 v[54:55], v[54:55], v[110:111], v[110:111] op_sel_hi:[1,0,0]\n"
"v_pk_fma_f32 v[56:57], v[56:57], v[110:111], v[110:111] op_sel_hi:[1,0,0]\n"
"v_pk_fma_f32 v[58:59], v[58:59], v[110:111], v[110:111] op_sel_hi:[1,0,0]\n"
"v_pk_fma_f32 v[60:61], v[60:61], v[110:111], v[110:111] op_sel_hi:[1,0,0]\n"
"v_rcp_f32 v54, v54\n"
"v_pk_fma_f32 v[136:137], v[64:65], v[100:101], v[136:137] op_sel_hi:[1,0,1] neg_lo:[0,1,0] neg_hi:[0,1,0]\n"
"v_rcp_f32 v55, v55\n"
"v_pk_fma_f32 v[138:139], v[66:67], v[100:101], v[138:139] op_sel_hi:[1,0,1] neg_lo:[0,1,0] neg_hi:[0,1,0]\n"
"v_rcp_f32 v56, v56\n"
"v_pk_fma_f32 v[152:153], v[68:69], v[100:101], v[152:153] op_sel_hi:[1,0,1] neg_lo:[0,1,0] neg_hi:[0,1,0]\n"
"v_rcp_f32 v57, v57\n"
"v_pk_fma_f32 v[154:155], v[70:71], v[100:101], v[154:155] op_sel_hi:[1,0,1] neg_lo:[0,1,0] neg_hi:[0,1,0]\n"
"v_rcp_f32 v58, v58\n"
"v_pk_mul_f32 v[16:17], v[136:137], v[136:137]\n"
"v_rcp_f32 v59, v59\n"
"v_pk_mul_f32 v[18:19], v[138:139], v[138:139]\n"
"v_rcp_f32 v60, v60\n"
"v_pk_mul_f32 v[20:21], v[152:153], v[152:153]\n"
"v_rcp_f32 v61, v61\n"
"v_pk_mul_f32 v[22:23], v[154:155], v[154:155]\n"
"v_pk_mul_f32 v[196:197], v[196:197], v[54:55]\n"
"v_pk_mul_f32 v[198:199], v[198:199], v[56:57]\n"
"v_pk_mul_f32 v[212:213], v[212:213], v[58:59]\n"
"v_pk_mul_f32 v[214:215], v[214:215], v[60:61]\n"
"v_pk_fma_f32 v[16:17], v[112:113], v[16:17], v[112:113] op_sel:[1,0,0] op_sel_hi:[1,1,0]\n"
"v_pk_fma_f32 v[18:19], v[112:113], v[18:19], v[112:113] op_sel:[1,0,0] op_sel_hi:[1,1,0]\n"
"v_pk_fma_f32 v[20:21], v[112:113], v[20:21], v[112:113] op_sel:[1,0,0] op_sel_hi:[1,1,0]\n"
"v_pk_fma_f32 v[22:23], v[112:113], v[22:23], v[112:113] op_sel:[1,0,0] op_sel_hi:[1,1,0]\n"
"v_cvt_pk_bf16_f32 v4, v196, v197\n"
"v_cvt_pk_bf16_f32 v5, v198, v199\n"
"v_cvt_pk_bf16_f32 v6, v212, v213\n"
"v_cvt_pk_bf16_f32 v7, v214, v215\n"
"buffer_store_dwordx4 v[4:7], v52, s[88:91], s53 offen offset:0 sc1\n"
"v_pk_mul_f32 v[16:17], v[136:137], v[16:17]\n"
"v_pk_mul_f32 v[18:19], v[138:139], v[18:19]\n"
"v_pk_mul_f32 v[20:21], v[152:153], v[20:21]\n"
"s_waitcnt vmcnt(2)\n"
"v_cmp_ne_u64 vcc, s[34:35], v[62:63]\n"
"s_nop 4\n"
"s_cbranch_vccnz .L_slow_u%=\n"
".L_back_u%=:\n"
"v_pk_mul_f32 v[22:23], v[154:155], v[22:23]\n"
"v_exp_f32 v16, v16\n"
"s_lshl_b32 s28, s6, 8\n"
"v_pk_fma_f32 v[200:201], v[80:81], v[100:101], v[200:201] op_sel_hi:[1,0,1] neg_lo:[0,1,0] neg_hi:[0,1,0]\n"
"v_exp_f32 v17, v17\n"
"v_pk_fma_f32 v[202:203], v[82:83], v[100:101], v[202:203] op_sel_hi:[1,0,1] neg_lo:[0,1,0] neg_hi:[0,1,0]\n"
"s_and_b32 s29, s7, 2\n"
"v_exp_f32 v18, v18\n"
"v_pk_fma_f32 v[216:217], v[84:85], v[100:101], v[216:217] op_sel_hi:[1,0,1] neg_lo:[0,1,0] neg_hi:[0,1,0]\n"
"s_lshl_b32 s29, s29, 6\n"
"v_exp_f32 v19, v19\n"
"v_pk_fma_f32 v[218:219], v[86:87], v[100:101], v[218:219] op_sel_hi:[1,0,1] neg_lo:[0,1,0] neg_hi:[0,1,0]\n"
"v_exp_f32 v20, v20\n"
"s_add_u32 s28, s28, s29\n"
"v_pk_mul_f32 v[200:201], v[136:137], v[200:201]\n"
"v_exp_f32 v21, v21\n"
"s_and_b32 s29, s7, 1\n"
"v_pk_mul_f32 v[202:203], v[138:139], v[202:203]\n"
"v_exp_f32 v22, v22\n"
"v_pk_mul_f32 v[216:217], v[152:153], v[216:217]\n"
"s_mul_i32 s29, s29, 1052672\n"
"v_exp_f32 v23, v23\n"
"v_pk_mul_f32 v[218:219], v[154:155], v[218:219]\n"
"s_add_u32 s28, s28, s29\n"
"v_pk_fma_f32 v[16:17], v[16:17], v[114:115], v[114:115] op_sel_hi:[1,0,0]\n"
"v_pk_fma_f32 v[18:19], v[18:19], v[114:115], v[114:115] op_sel_hi:[1,0,0]\n"
"v_pk_fma_f32 v[20:21], v[20:21], v[114:115], v[114:115] op_sel_hi:[1,0,0]\n"
"s_add_u32 s28, s28, 0x100000\n"
"v_pk_fma_f32 v[22:23], v[22:23], v[114:115], v[114:115] op_sel_hi:[1,0,0]\n"
"v_rcp_f32 v16, v16\n"
"v_pk_fma_f32 v[140:141], v[64:65], v[102:103], v[140:141] op_sel_hi:[1,0,1] neg_lo:[0,1,0] neg_hi:[0,1,0]\n"
"s_add_u32 s29, s28, 526336\n"
"v_rcp_f32 v17, v17\n"
"v_pk_fma_f32 v[142:143], v[66:67], v[102:103], v[142:143] op_sel_hi:[1,0,1] neg_lo:[0,1,0] neg_hi:[0,1,0]\n"
"v_lshrrev_b32 v28, 3, v0\n"
"v_rcp_f32 v18, v18\n"
"v_pk_fma_f32 v[156:157], v[68:69], v[102:103], v[156:157] op_sel_hi:[1,0,1] neg_lo:[0,1,0] neg_hi:[0,1,0]\n"
"v_rcp_f32 v19, v19\n"
"v_mul_u32_u24 v28, 65792, v28\n"
"v_pk_fma_f32 v[158:159], v[70:71], v[102:103], v[158:159] op_sel_hi:[1,0,1] neg_lo:[0,1,0] neg_hi:[0,1,0]\n"
"v_rcp_f32 v20, v20\n"
"v_and_b32 v62, 7, v0\n"
"v_pk_mul_f32 v[54:55], v[140:141], v[140:141]\n"
"v_rcp_f32 v21, v21\n"
"v_pk_mul_f32 v[56:57], v[142:143], v[142:143]\n"
"v_lshl_add_u32 v28, v62, 4, v28\n"
"v_rcp_f32 v22, v22\n"
"v_pk_mul_f32 v[58:59], v[156:157], v[156:157]\n"
"buffer_load_dwordx4 a[212:215], v28, s[16:19], s28 offen sc1\n"
"v_rcp_f32 v23, v23\n"
"v_pk_mul_f32 v[60:61], v[158:159], v[158:159]\n"
"v_pk_mul_f32 v[200:201], v[200:201], v[16:17]\n"
"buffer_load_dwordx4 a[216:219], v28, s[16:19], s29 offen sc1\n"
"v_pk_mul_f32 v[202:203], v[202:203], v[18:19]\n"
"v_pk_mul_f32 v[216:217], v[216:217], v[20:21]\n"
"v_pk_mul_f32 v[218:219], v[218:219], v[22:23]\n"
"v_pk_fma_f32 v[54:55], v[116:117], v[54:55], v[116:117] op_sel:[1,0,0] op_sel_hi:[1,1,0]\n"
"v_pk_fma_f32 v[56:57], v[116:117], v[56:57], v[116:117] op_sel:[1,0,0] op_sel_hi:[1,1,0]\n"
"v_pk_fma_f32 v[58:59], v[116:117], v[58:59], v[116:117] op_sel:[1,0,0] op_sel_hi:[1,1,0]\n"
"v_pk_fma_f32 v[60:61], v[116:117], v[60:61], v[116:117] op_sel:[1,0,0] op_sel_hi:[1,1,0]\n"
"v_cvt_pk_bf16_f32 v4, v200, v201\n"
"v_cvt_pk_bf16_f32 v5, v202, v203\n"
"v_cvt_pk_bf16_f32 v6, v216, v217\n"
"v_cvt_pk_bf16_f32 v7, v218, v219\n"
"buffer_store_dwordx4 v[4:7], v52, s[88:91], s54 offen offset:0 sc1\n"
"v_pk_mul_f32 v[54:55], v[140:141], v[54:55]\n"
"v_pk_mul_f32 v[56:57], v[142:143], v[56:57]\n"
"v_pk_mul_f32 v[58:59], v[156:157], v[58:59]\n"
"v_pk_mul_f32 v[60:61], v[158:159], v[60:61]\n"
"v_exp_f32 v54, v54\n"
"v_pk_fma_f32 v[204:205], v[80:81], v[102:103], v[204:205] op_sel_hi:[1,0,1] neg_lo:[0,1,0] neg_hi:[0,1,0]\n"
"v_exp_f32 v55, v55\n"
"v_pk_fma_f32 v[206:207], v[82:83], v[102:103], v[206:207] op_sel_hi:[1,0,1] neg_lo:[0,1,0] neg_hi:[0,1,0]\n"
"v_exp_f32 v56, v56\n"
"v_pk_fma_f32 v[220:221], v[84:85], v[102:103], v[220:221] op_sel_hi:[1,0,1] neg_lo:[0,1,0] neg_hi:[0,1,0]\n"
"s_lshl_b32 s28, s48, 6\n"
"v_exp_f32 v57, v57\n"
"v_pk_fma_f32 v[222:223], v[86:87], v[102:103], v[222:223] op_sel_hi:[1,0,1] neg_lo:[0,1,0] neg_hi:[0,1,0]\n"
"v_exp_f32 v58, v58\n"
"v_pk_mul_f32 v[204:205], v[140:141], v[204:205]\n"
"s_lshl_b32 s29, s5, 4\n"
"v_exp_f32 v59, v59\n"
"v_pk_mul_f32 v[206:207], v[142:143], v[206:207]\n"
"v_exp_f32 v60, v60\n"
"v_pk_mul_f32 v[220:221], v[156:157], v[220:221]\n"
"s_add_u32 s28, s28, s29\n"
"v_exp_f32 v61, v61\n"
"v_pk_mul_f32 v[222:223], v[158:159], v[222:223]\n"
"v_pk_fma_f32 v[54:55], v[54:55], v[118:119], v[118:119] op_sel_hi:[1,0,0]\n"
"v_pk_fma_f32 v[56:57], v[56:57], v[118:119], v[118:119] op_sel_hi:[1,0,0]\n"
"v_pk_fma_f32 v[58:59], v[58:59], v[118:119], v[118:119] op_sel_hi:[1,0,0]\n"
"v_add_u32 v3, s28, v2\n"
"v_pk_fma_f32 v[60:61], v[60:61], v[118:119], v[118:119] op_sel_hi:[1,0,0]\n"
"v_rcp_f32 v54, v54\n"
"v_pk_fma_f32 v[160:161], v[72:73], v[96:97], v[160:161] op_sel_hi:[1,0,1] neg_lo:[0,1,0] neg_hi:[0,1,0]\n"
"v_rcp_f32 v55, v55\n"
"v_lshlrev_b32 v3, 3, v3\n"
"v_pk_fma_f32 v[162:163], v[74:75], v[96:97], v[162:163] op_sel_hi:[1,0,1] neg_lo:[0,1,0] neg_hi:[0,1,0]\n"
"v_rcp_f32 v56, v56\n"
"v_pk_fma_f32 v[176:177], v[76:77], v[96:97], v[176:177] op_sel_hi:[1,0,1] neg_lo:[0,1,0] neg_hi:[0,1,0]\n"
"v_rcp_f32 v57, v57\n"
"v_lshrrev_b32 v36, 2, v0\n"
"v_pk_fma_f32 v[178:179], v[78:79], v[96:97], v[178:179] op_sel_hi:[1,0,1] neg_lo:[0,1,0] neg_hi:[0,1,0]\n"
"v_rcp_f32 v58, v58\n"
"v_pk_mul_f32 v[16:17], v[160:161], v[160:161]\n"
"v_rcp_f32 v59, v59\n"
"v_pk_mul_f32 v[18:19], v[162:163], v[162:163]\n"
"v_add_u32 v50, s28, v36\n"
"v_rcp_f32 v60, v60\n"
"v_pk_mul_f32 v[20:21], v[176:177], v[176:177]\n"
"v_rcp_f32 v61, v61\n"
"v_pk_mul_f32 v[22:23], v[178:179], v[178:179]\n"
"v_lshlrev_b32 v50, 10, v50\n"
"v_pk_mul_f32 v[204:205], v[204:205], v[54:55]\n"
"v_pk_mul_f32 v[206:207], v[206:207], v[56:57]\n"
"v_pk_mul_f32 v[220:221], v[220:221], v[58:59]\n"
"v_pk_mul_f32 v[222:223], v[222:223], v[60:61]\n"
"v_pk_fma_f32 v[16:17], v[104:105], v[16:17], v[104:105] op_sel:[1,0,0] op_sel_hi:[1,1,0]\n"
"v_and_b32 v37, 3, v0\n"
"v_pk_fma_f32 v[18:19], v[104:105], v[18:19], v[104:105] op_sel:[1,0,0] op_sel_hi:[1,1,0]\n"
"v_pk_fma_f32 v[20:21], v[104:105], v[20:21], v[104:105] op_sel:[1,0,0] op_sel_hi:[1,1,0]\n"
"v_pk_fma_f32 v[22:23], v[104:105], v[22:23], v[104:105] op_sel:[1,0,0] op_sel_hi:[1,1,0]\n"
"v_cvt_pk_bf16_f32 v4, v204, v205\n"
"v_lshl_add_u32 v50, v37, 4, v50\n"
"v_cvt_pk_bf16_f32 v5, v206, v207\n"
"v_cvt_pk_bf16_f32 v6, v220, v221\n"
"v_cvt_pk_bf16_f32 v7, v222, v223\n"
"buffer_store_dwordx4 v[4:7], v52, s[88:91], s55 offen offset:0 sc1\n"
"s_lshl_b32 s59, s49, 7\n"
"v_pk_mul_f32 v[16:17], v[160:161], v[16:17]\n"
"v_pk_mul_f32 v[18:19], v[162:163], v[18:19]\n"
"v_pk_mul_f32 v[20:21], v[176:177], v[20:21]\n"
"v_pk_mul_f32 v[22:23], v[178:179], v[22:23]\n"
"v_exp_f32 v16, v16\n"
"s_mov_b32 s28, 0x10000\n"
"v_pk_fma_f32 v[224:225], v[88:89], v[96:97], v[224:225] op_sel_hi:[1,0,1] neg_lo:[0,1,0] neg_hi:[0,1,0]\n"
"v_exp_f32 v17, v17\n"
"v_pk_fma_f32 v[226:227], v[90:91], v[96:97], v[226:227] op_sel_hi:[1,0,1] neg_lo:[0,1,0] neg_hi:[0,1,0]\n"
"v_exp_f32 v18, v18\n"
"buffer_load_dwordx2 v[48:49], v3, s[16:19], s28 offen sc1\n"
"v_pk_fma_f32 v[240:241], v[92:93], v[96:97], v[240:241] op_sel_hi:[1,0,1] neg_lo:[0,1,0] neg_hi:[0,1,0]\n"
"v_exp_f32 v19, v19\n"
"v_pk_fma_f32 v[242:243], v[94:95], v[96:97], v[242:243] op_sel_hi:[1,0,1] neg_lo:[0,1,0] neg_hi:[0,1,0]\n"
"v_exp_f32 v20, v20\n"
"v_pk_mul_f32 v[224:225], v[160:161], v[224:225]\n"
"buffer_load_dwordx4 v[40:43], v50, s[8:11], s59 offen offset:0\n"
"v_exp_f32 v21, v21\n"
"v_pk_mul_f32 v[226:227], v[162:163], v[226:227]\n"
"v_exp_f32 v22, v22\n"
"v_pk_mul_f32 v[240:241], v[176:177], v[240:241]\n"
"buffer_load_dwordx4 v[44:47], v50, s[8:11], s59 offen offset:64\n"
"v_exp_f32 v23, v23\n"
"v_pk_mul_f32 v[242:243], v[178:179], v[242:243]\n"
"v_pk_fma_f32 v[16:17], v[16:17], v[106:107], v[106:107] op_sel_hi:[1,0,0]\n"
"v_pk_fma_f32 v[18:19], v[18:19], v[106:107], v[106:107] op_sel_hi:[1,0,0]\n"
"v_mul_u32_u24 v36, 144, v36\n"
"v_pk_fma_f32 v[20:21], v[20:21], v[106:107], v[106:107] op_sel_hi:[1,0,0]\n"
"v_pk_fma_f32 v[22:23], v[22:23], v[106:107], v[106:107] op_sel_hi:[1,0,0]\n"
"v_rcp_f32 v16, v16\n"
"v_pk_fma_f32 v[164:165], v[72:73], v[98:99], v[164:165] op_sel_hi:[1,0,1] neg_lo:[0,1,0] neg_hi:[0,1,0]\n"
"v_rcp_f32 v17, v17\n"
"v_lshl_add_u32 v36, v37, 4, v36\n"
"v_pk_fma_f32 v[166:167], v[74:75], v[98:99], v[166:167] op_sel_hi:[1,0,1] neg_lo:[0,1,0] neg_hi:[0,1,0]\n"
"v_rcp_f32 v18, v18\n"
"v_pk_fma_f32 v[180:181], v[76:77], v[98:99], v[180:181] op_sel_hi:[1,0,1] neg_lo:[0,1,0] neg_hi:[0,1,0]\n"
"v_rcp_f32 v19, v19\n"
"s_mul_i32 s28, s7, 2304\n"
"v_pk_fma_f32 v[182:183], v[78:79], v[98:99], v[182:183] op_sel_hi:[1,0,1] neg_lo:[0,1,0] neg_hi:[0,1,0]\n"
"v_rcp_f32 v20, v20\n"
"v_pk_mul_f32 v[54:55], v[164:165], v[164:165]\n"
"v_rcp_f32 v21, v21\n"
"v_pk_mul_f32 v[56:57], v[166:167], v[166:167]\n"
"s_add_u32 s28, s28, 94464\n"
"v_rcp_f32 v22, v22\n"
"v_pk_mul_f32 v[58:59], v[180:181], v[180:181]\n"
"v_rcp_f32 v23, v23\n"
"v_pk_mul_f32 v[60:61], v[182:183], v[182:183]\n"
"v_add_u32 v36, s28, v36\n"
"v_pk_mul_f32 v[224:225], v[224:225], v[16:17]\n"
"v_pk_mul_f32 v[226:227], v[226:227], v[18:19]\n"
"v_pk_mul_f32 v[240:241], v[240:241], v[20:21]\n"
"v_pk_mul_f32 v[242:243], v[242:243], v[22:23]\n"
"v_mul_u32_u24 v37, 144, v2\n"
"v_pk_fma_f32 v[54:55], v[108:109], v[54:55], v[108:109] op_sel:[1,0,0] op_sel_hi:[1,1,0]\n"
"v_pk_fma_f32 v[56:57], v[108:109], v[56:57], v[108:109] op_sel:[1,0,0] op_sel_hi:[1,1,0]\n"
"v_pk_fma_f32 v[58:59], v[108:109], v[58:59], v[108:109] op_sel:[1,0,0] op_sel_hi:[1,1,0]\n"
"v_pk_fma_f32 v[60:61], v[108:109], v[60:61], v[108:109] op_sel:[1,0,0] op_sel_hi:[1,1,0]\n"
"v_cvt_pk_bf16_f32 v4, v224, v225\n"
"v_lshl_add_u32 v37, v1, 3, v37\n"
"v_cvt_pk_bf16_f32 v5, v226, v227\n"
"v_cvt_pk_bf16_f32 v6, v240, v241\n"
"v_cvt_pk_bf16_f32 v7, v242, v243\n"
"buffer_store_dwordx4 v[4:7], v52, s[88:91], s52 offen offset:1024 sc1\n"
"v_add_u32 v37, s28, v37\n"
"v_pk_mul_f32 v[54:55], v[164:165], v[54:55]\n"
"v_pk_mul_f32 v[56:57], v[166:167], v[56:57]\n"
"v_pk_mul_f32 v[58:59], v[180:181], v[58:59]\n"
"v_pk_mul_f32 v[60:61], v[182:183], v[60:61]\n"
"v_exp_f32 v54, v54\n"
"v_pk_fma_f32 v[228:229], v[88:89], v[98:99], v[228:229] op_sel_hi:[1,0,1] neg_lo:[0,1,0] neg_hi:[0,1,0]\n"
"v_exp_f32 v55, v55\n"
"v_pk_fma_f32 v[230:231], v[90:91], v[98:99], v[230:231] op_sel_hi:[1,0,1] neg_lo:[0,1,0] neg_hi:[0,1,0]\n"
"v_exp_f32 v56, v56\n"
"v_pk_fma_f32 v[244:245], v[92:93], v[98:99], v[244:245] op_sel_hi:[1,0,1] neg_lo:[0,1,0] neg_hi:[0,1,0]\n"
"v_exp_f32 v57, v57\n"
"v_pk_fma_f32 v[246:247], v[94:95], v[98:99], v[246:247] op_sel_hi:[1,0,1] neg_lo:[0,1,0] neg_hi:[0,1,0]\n"
"v_exp_f32 v58, v58\n"
"v_pk_mul_f32 v[228:229], v[164:165], v[228:229]\n"
"v_exp_f32 v59, v59\n"
"v_pk_mul_f32 v[230:231], v[166:167], v[230:231]\n"
"v_exp_f32 v60, v60\n"
"v_pk_mul_f32 v[244:245], v[180:181], v[244:245]\n"
"v_exp_f32 v61, v61\n"
"v_pk_mul_f32 v[246:247], v[182:183], v[246:247]\n"
"v_pk_fma_f32 v[54:55], v[54:55], v[110:111], v[110:111] op_sel_hi:[1,0,0]\n"
"v_pk_fma_f32 v[56:57], v[56:57], v[110:111], v[110:111] op_sel_hi:[1,0,0]\n"
"v_pk_fma_f32 v[58:59], v[58:59], v[110:111], v[110:111] op_sel_hi:[1,0,0]\n"
"v_pk_fma_f32 v[60:61], v[60:61], v[110:111], v[110:111] op_sel_hi:[1,0,0]\n"
"v_rcp_f32 v54, v54\n"
"v_pk_fma_f32 v[168:169], v[72:73], v[100:101], v[168:169] op_sel_hi:[1,0,1] neg_lo:[0,1,0] neg_hi:[0,1,0]\n"
"v_rcp_f32 v55, v55\n"
"v_pk_fma_f32 v[170:171], v[74:75], v[100:101], v[170:171] op_sel_hi:[1,0,1] neg_lo:[0,1,0] neg_hi:[0,1,0]\n"
"v_rcp_f32 v56, v56\n"
"v_pk_fma_f32 v[184:185], v[76:77], v[100:101], v[184:185] op_sel_hi:[1,0,1] neg_lo:[0,1,0] neg_hi:[0,1,0]\n"
"v_rcp_f32 v57, v57\n"
"v_pk_fma_f32 v[186:187], v[78:79], v[100:101], v[186:187] op_sel_hi:[1,0,1] neg_lo:[0,1,0] neg_hi:[0,1,0]\n"
"v_rcp_f32 v58, v58\n"
"v_pk_mul_f32 v[16:17], v[168:169], v[168:169]\n"
"v_rcp_f32 v59, v59\n"
"v_pk_mul_f32 v[18:19], v[170:171], v[170:171]\n"
"v_rcp_f32 v60, v60\n"
"v_pk_mul_f32 v[20:21], v[184:185], v[184:185]\n"
"v_rcp_f32 v61, v61\n"
"v_pk_mul_f32 v[22:23], v[186:187], v[186:187]\n"
"v_pk_mul_f32 v[228:229], v[228:229], v[54:55]\n"
"v_pk_mul_f32 v[230:231], v[230:231], v[56:57]\n"
"v_pk_mul_f32 v[244:245], v[244:245], v[58:59]\n"
"v_pk_mul_f32 v[246:247], v[246:247], v[60:61]\n"
"v_pk_fma_f32 v[16:17], v[112:113], v[16:17], v[112:113] op_sel:[1,0,0] op_sel_hi:[1,1,0]\n"
"v_pk_fma_f32 v[18:19], v[112:113], v[18:19], v[112:113] op_sel:[1,0,0] op_sel_hi:[1,1,0]\n"
"v_pk_fma_f32 v[20:21], v[112:113], v[20:21], v[112:113] op_sel:[1,0,0] op_sel_hi:[1,1,0]\n"
"v_pk_fma_f32 v[22:23], v[112:113], v[22:23], v[112:113] op_sel:[1,0,0] op_sel_hi:[1,1,0]\n"
"v_cvt_pk_bf16_f32 v4, v228, v229\n"
"v_cvt_pk_bf16_f32 v5, v230, v231\n"
"v_cvt_pk_bf16_f32 v6, v244, v245\n"
"v_cvt_pk_bf16_f32 v7, v246, v247\n"
"buffer_store_dwordx4 v[4:7], v52, s[88:91], s53 offen offset:1024 sc1\n"
"v_pk_mul_f32 v[16:17], v[168:169], v[16:17]\n"
"v_pk_mul_f32 v[18:19], v[170:171], v[18:19]\n"
"v_pk_mul_f32 v[20:21], v[184:185], v[20:21]\n"
"v_pk_mul_f32 v[22:23], v[186:187], v[22:23]\n"
"v_exp_f32 v16, v16\n"
"v_pk_fma_f32 v[232:233], v[88:89], v[100:101], v[232:233] op_sel_hi:[1,0,1] neg_lo:[0,1,0] neg_hi:[0,1,0]\n"
"v_exp_f32 v17, v17\n"
"v_pk_fma_f32 v[234:235], v[90:91], v[100:101], v[234:235] op_sel_hi:[1,0,1] neg_lo:[0,1,0] neg_hi:[0,1,0]\n"
"v_exp_f32 v18, v18\n"
"v_pk_fma_f32 v[248:249], v[92:93], v[100:101], v[248:249] op_sel_hi:[1,0,1] neg_lo:[0,1,0] neg_hi:[0,1,0]\n"
"v_exp_f32 v19, v19\n"
"v_pk_fma_f32 v[250:251], v[94:95], v[100:101], v[250:251] op_sel_hi:[1,0,1] neg_lo:[0,1,0] neg_hi:[0,1,0]\n"
"v_exp_f32 v20, v20\n"
"v_pk_mul_f32 v[232:233], v[168:169], v[232:233]\n"
"v_exp_f32 v21, v21\n"
"v_pk_mul_f32 v[234:235], v[170:171], v[234:235]\n"
"v_exp_f32 v22, v22\n"
"v_pk_mul_f32 v[248:249], v[184:185], v[248:249]\n"
"v_exp_f32 v23, v23\n"
"v_pk_mul_f32 v[250:251], v[186:187], v[250:251]\n"
"v_pk_fma_f32 v[16:17], v[16:17], v[114:115], v[114:115] op_sel_hi:[1,0,0]\n"
"v_pk_fma_f32 v[18:19], v[18:19], v[114:115], v[114:115] op_sel_hi:[1,0,0]\n"
"v_pk_fma_f32 v[20:21], v[20:21], v[114:115], v[114:115] op_sel_hi:[1,0,0]\n"
"v_pk_fma_f32 v[22:23], v[22:23], v[114:115], v[114:115] op_sel_hi:[1,0,0]\n"
"v_rcp_f32 v16, v16\n"
"v_pk_fma_f32 v[172:173], v[72:73], v[102:103], v[172:173] op_sel_hi:[1,0,1] neg_lo:[0,1,0] neg_hi:[0,1,0]\n"
"v_rcp_f32 v17, v17\n"
"v_pk_fma_f32 v[174:175], v[74:75], v[102:103], v[174:175] op_sel_hi:[1,0,1] neg_lo:[0,1,0] neg_hi:[0,1,0]\n"
"v_rcp_f32 v18, v18\n"
"v_pk_fma_f32 v[188:189], v[76:77], v[102:103], v[188:189] op_sel_hi:[1,0,1] neg_lo:[0,1,0] neg_hi:[0,1,0]\n"
"v_rcp_f32 v19, v19\n"
"v_pk_fma_f32 v[190:191], v[78:79], v[102:103], v[190:191] op_sel_hi:[1,0,1] neg_lo:[0,1,0] neg_hi:[0,1,0]\n"
"v_rcp_f32 v20, v20\n"
"v_pk_mul_f32 v[54:55], v[172:173], v[172:173]\n"
"v_rcp_f32 v21, v21\n"
"v_pk_mul_f32 v[56:57], v[174:175], v[174:175]\n"
"v_rcp_f32 v22, v22\n"
"v_pk_mul_f32 v[58:59], v[188:189], v[188:189]\n"
"v_rcp_f32 v23, v23\n"
"v_pk_mul_f32 v[60:61], v[190:191], v[190:191]\n"
"v_pk_mul_f32 v[232:233], v[232:233], v[16:17]\n"
"v_pk_mul_f32 v[234:235], v[234:235], v[18:19]\n"
"v_pk_mul_f32 v[248:249], v[248:249], v[20:21]\n"
"v_pk_mul_f32 v[250:251], v[250:251], v[22:23]\n"
"v_pk_fma_f32 v[54:55], v[116:117], v[54:55], v[116:117] op_sel:[1,0,0] op_sel_hi:[1,1,0]\n"
"v_pk_fma_f32 v[56:57], v[116:117], v[56:57], v[116:117] op_sel:[1,0,0] op_sel_hi:[1,1,0]\n"
"v_pk_fma_f32 v[58:59], v[116:117], v[58:59], v[116:117] op_sel:[1,0,0] op_sel_hi:[1,1,0]\n"
"v_pk_fma_f32 v[60:61], v[116:117], v[60:61], v[116:117] op_sel:[1,0,0] op_sel_hi:[1,1,0]\n"
"v_cvt_pk_bf16_f32 v4, v232, v233\n"
"v_cvt_pk_bf16_f32 v5, v234, v235\n"
"v_cvt_pk_bf16_f32 v6, v248, v249\n"
"v_cvt_pk_bf16_f32 v7, v250, v251\n"
"buffer_store_dwordx4 v[4:7], v52, s[88:91], s54 offen offset:1024 sc1\n"
"v_pk_mul_f32 v[54:55], v[172:173], v[54:55]\n"
"v_pk_mul_f32 v[56:57], v[174:175], v[56:57]\n"
"v_pk_mul_f32 v[58:59], v[188:189], v[58:59]\n"
"v_pk_mul_f32 v[60:61], v[190:191], v[60:61]\n"
"v_exp_f32 v54, v54\n"
"v_pk_fma_f32 v[236:237], v[88:89], v[102:103], v[236:237] op_sel_hi:[1,0,1] neg_lo:[0,1,0] neg_hi:[0,1,0]\n"
"v_exp_f32 v55, v55\n"
"v_pk_fma_f32 v[238:239], v[90:91], v[102:103], v[238:239] op_sel_hi:[1,0,1] neg_lo:[0,1,0] neg_hi:[0,1,0]\n"
"v_exp_f32 v56, v56\n"
"v_pk_fma_f32 v[252:253], v[92:93], v[102:103], v[252:253] op_sel_hi:[1,0,1] neg_lo:[0,1,0] neg_hi:[0,1,0]\n"
"v_exp_f32 v57, v57\n"
"v_pk_fma_f32 v[254:255], v[94:95], v[102:103], v[254:255] op_sel_hi:[1,0,1] neg_lo:[0,1,0] neg_hi:[0,1,0]\n"
"s_and_b32 s28, s7, 1\n"
"v_exp_f32 v58, v58\n"
"v_pk_mul_f32 v[236:237], v[172:173], v[236:237]\n"
"v_exp_f32 v59, v59\n"
"s_lshl_b32 s28, s28, 11\n"
"v_pk_mul_f32 v[238:239], v[174:175], v[238:239]\n"
"v_exp_f32 v60, v60\n"
"v_pk_mul_f32 v[252:253], v[188:189], v[252:253]\n"
"s_add_u32 s28, s28, s87\n"
"v_exp_f32 v61, v61\n"
"v_pk_mul_f32 v[254:255], v[190:191], v[254:255]\n"
"v_pk_fma_f32 v[54:55], v[54:55], v[118:119], v[118:119] op_sel_hi:[1,0,0]\n"
"v_add_u32 v28, s28, v52\n"
"v_pk_fma_f32 v[56:57], v[56:57], v[118:119], v[118:119] op_sel_hi:[1,0,0]\n"
"v_pk_fma_f32 v[58:59], v[58:59], v[118:119], v[118:119] op_sel_hi:[1,0,0]\n"
"v_pk_fma_f32 v[60:61], v[60:61], v[118:119], v[118:119] op_sel_hi:[1,0,0]\n"
"s_waitcnt vmcnt(8)\n"
"v_rcp_f32 v54, v54\n"
"v_rcp_f32 v55, v55\n"
"v_rcp_f32 v56, v56\n"
"ds_write_b128 v28, a[212:215] offset:0\n"
"v_rcp_f32 v57, v57\n"
"v_rcp_f32 v58, v58\n"
"v_rcp_f32 v59, v59\n"
"ds_write_b128 v28, a[216:219] offset:1024\n"
"v_rcp_f32 v60, v60\n"
"v_rcp_f32 v61, v61\n"
"v_pk_mul_f32 v[236:237], v[236:237], v[54:55]\n"
"v_pk_mul_f32 v[238:239], v[238:239], v[56:57]\n"
"v_pk_mul_f32 v[252:253], v[252:253], v[58:59]\n"
"v_pk_mul_f32 v[254:255], v[254:255], v[60:61]\n"
"v_cvt_pk_bf16_f32 v4, v236, v237\n"
"v_cvt_pk_bf16_f32 v5, v238, v239\n"
"v_cvt_pk_bf16_f32 v6, v252, v253\n"
"v_cvt_pk_bf16_f32 v7, v254, v255\n"
"buffer_store_dwordx4 v[4:7], v52, s[88:91], s55 offen offset:1024 sc1\n"
"s_branch .L_common%=\n"
".L_prep%=:\n"
"s_sub_u32 s27, s23, 28\n"
"s_and_b32 s20, s22, 3\n"
"s_lshl_b32 s24, s20, 3\n"
"s_lshr_b32 s21, s22, 2\n"
"s_lshl_b32 s25, s21, 2\n"
"s_add_u32 s24, s24, s25\n"
"s_add_u32 s52, s24, s27\n"
"s_lshl_b32 s53, s52, 6\n"
"s_lshl_b32 s24, s7, 4\n"
"s_add_u32 s53, s53, s24\n"
"s_movk_i32 s54, 0x1000\n"
"s_movk_i32 s55, 0x2000\n"
"s_movk_i32 s4, 0x3000\n"
"v_add_u32 v10, s53, v1\n"
"v_lshlrev_b32 v10, 10, v10\n"
"v_lshl_add_u32 v10, v2, 4, v10\n"
"buffer_load_dwordx4 v[128:131], v10, s[8:11], 0 offen offset:0\n"
"buffer_load_dwordx4 v[132:135], v10, s[8:11], 0 offen offset:256\n"
"s_mov_b32 s16, s84\n"
"s_and_b32 s17, s85, 0xffff\n"
"s_mov_b32 s18, 0xd00000\n"
"s_mov_b32 s19, 0x20000\n"
"v_mov_b32 v30, 0\n"
"buffer_load_dwordx2 v[32:33], v30, s[16:19], 0 offen sc1\n"
"s_cmp_lg_u32 s7, 0\n"
"s_cbranch_scc1 .L_nopf2_%=\n"
"s_getpc_b64 s[28:29]\n"
".L_pc2_%=:\n"
"s_sub_u32 s28, s28, .L_pc2_%=-.L_start%=\n"
"s_subb_u32 s29, s29, 0\n"
"s_and_b32 s30, s23, 7\n"
"s_lshl_b32 s30, s30, 13\n"
"v_lshl_add_u32 v31, v0, 7, s30\n"
"v_min_u32 v31, .L_end%=-.L_start%=-4, v31\n"
"global_load_dword v34, v31, s[28:29]\n"
".L_nopf2_%=:\n"
"s_mov_b32 s36, s72\n"
"s_and_b32 s37, s73, 0xffff\n"
"s_mov_b32 s38, 0x200000\n"
"s_mov_b32 s39, 0x20000\n"
"buffer_load_dwordx4 v[136:139], v10, s[8:11], 0 offen offset:512\n"
"s_mov_b32 s40, s80\n"
"s_and_b32 s41, s81, 0xffff\n"
"s_mov_b32 s42, 0x1c0000\n"
"s_mov_b32 s43, 0x20000\n"
"buffer_load_dwordx4 v[140:143], v10, s[8:11], 0 offen offset:768\n"
"s_mov_b32 s44, s82\n"
"s_and_b32 s45, s83, 0xffff\n"
"s_mov_b32 s46, 0x200000\n"
"s_mov_b32 s47, 0x20000\n"
"buffer_load_dwordx4 v[144:147], v10, s[8:11], s54 offen offset:0\n"
"s_mov_b32 s88, s84\n"
"s_and_b32 s89, s85, 0xffff\n"
"s_mov_b32 s90, 0xd00000\n"
"s_mov_b32 s91, 0x20000\n"
"buffer_load_dwordx4 v[148:151], v10, s[8:11], s54 offen offset:256\n"
"s_mov_b32 s92, s66\n"
"s_and_b32 s93, s67, 0xffff\n"
"s_mov_b32 s94, 0x200000\n"
"s_mov_b32 s95, 0x20000\n"
"buffer_load_dwordx4 v[152:155], v10, s[8:11], s54 offen offset:512\n"
"s_mov_b32 s96, s68\n"
"s_and_b32 s97, s69, 0xffff\n"
"s_mov_b32 s98, 0x200000\n"
"s_mov_b32 s99, 0x20000\n"
"buffer_load_dwordx4 v[156:159], v10, s[8:11], s54 offen offset:768\n"
"s_mov_b64 s[50:51], s[74:75]\n"
"s_mov_b64 s[56:57], s[70:71]\n"
"s_and_b32 s48, s22, 3\n"
"s_lshl_b32 s48, s48, 3\n"
"buffer_load_dwordx4 v[160:163], v10, s[8:11], s55 offen offset:0\n"
"s_lshr_b32 s28, s22, 2\n"
"s_lshl_b32 s28, s28, 2\n"
"s_add_u32 s48, s48, s28\n"
"s_and_b32 s28, s23, 3\n"
"buffer_load_dwordx4 v[164:167], v10, s[8:11], s55 offen offset:256\n"
"s_add_u32 s48, s48, s28\n"
"s_lshr_b32 s49, s23, 2\n"
"s_and_b32 s31, s48, 3\n"
"s_lshl_b32 s86, s31, 17\n"
"buffer_load_dwordx4 v[168:171], v10, s[8:11], s55 offen offset:512\n"
"s_mov_b32 s90, 0\n"
"s_lshl_b32 s24, s20, 2\n"
"s_add_u32 s24, s24, s27\n"
"s_cmp_ge_u32 s24, 7\n"
"s_cselect_b32 s12, s78, s76\n"
"s_cselect_b32 s13, s79, s77\n"
"s_cselect_b32 s25, 7, 0\n"
"s_cselect_b32 s20, 14, 0\n"
"buffer_load_dwordx4 v[172:175], v10, s[8:11], s55 offen offset:768\n"
"s_and_b32 s13, s13, 0xffff\n"
"s_mov_b32 s15, 0x20000\n"
"s_cmp_ge_u32 s24, 14\n"
"s_cselect_b32 s14, 0, 0x1c0000\n"
"s_cselect_b32 s23, 0, 1\n"
"s_sub_u32 s24, s24, s25\n"
"buffer_load_dwordx4 v[176:179], v10, s[8:11], s4 offen offset:0\n"
"s_mul_i32 s21, s21, 7\n"
"s_add_u32 s21, s21, s24\n"
"s_add_u32 s20, s20, s21\n"
"s_lshl_b32 s24, s7, 7\n"
"buffer_load_dwordx4 v[180:183], v10, s[8:11], s4 offen offset:256\n"
"v_add_u32 v4, s24, v1\n"
"v_mul_u32_u24 v4, 0xe00, v4\n"
"v_lshl_add_u32 v4, v2, 4, v4\n"
"s_lshl_b32 s27, s21, 8\n"
"buffer_load_dwordx4 v[184:187], v10, s[8:11], s4 offen offset:512\n"
"v_mov_b32 v12, 0x3f80\n"
"v_mov_b32 v13, 0x3f800000\n"
"s_mov_b32 s25, 0x3f803f80\n"
"v_cmp_eq_u32 vcc, 0, v2\n"
"s_nop 1\n"
"v_cndmask_b32 v44, 0, v12, vcc\n"
"v_cmp_eq_u32 vcc, 1, v2\n"
"s_nop 1\n"
"v_cndmask_b32 v44, v44, v13, vcc\n"
"buffer_load_dwordx4 v[188:191], v10, s[8:11], s4 offen offset:768\n"
"v_cmp_eq_u32 vcc, 2, v2\n"
"s_nop 1\n"
"v_cndmask_b32 v45, 0, v12, vcc\n"
"v_cmp_eq_u32 vcc, 3, v2\n"
"s_nop 1\n"
"v_cndmask_b32 v45, v45, v13, vcc\n"
"v_cmp_eq_u32 vcc, 4, v2\n"
"s_nop 1\n"
"v_cndmask_b32 v46, 0, v12, vcc\n"
"v_cmp_eq_u32 vcc, 5, v2\n"
"s_nop 1\n"
"v_cndmask_b32 v46, v46, v13, vcc\n"
"v_cmp_eq_u32 vcc, 6, v2\n"
"s_nop 1\n"
"v_cndmask_b32 v47, 0, v12, vcc\n"
"v_cmp_eq_u32 vcc, 7, v2\n"
"s_nop 1\n"
"v_cndmask_b32 v47, v47, v13, vcc\n"
"buffer_load_dwordx4 a[0:3], v4, s[12:15], s27 offen\n"
"s_add_u32 s27, s27, 0x3800\n"
"buffer_load_dwordx4 a[4:7], v4, s[12:15], s27 offen\n"
"s_add_u32 s27, s27, 0x3800\n"
"buffer_load_dwordx4 a[8:11], v4, s[12:15], s27 offen\n"
"s_add_u32 s27, s27, 0x3800\n"
"buffer_load_dwordx4 a[12:15], v4, s[12:15], s27 offen\n"
"s_add_u32 s27, s27, 0x3800\n"
"buffer_load_dwordx4 a[16:19], v4, s[12:15], s27 offen\n"
"s_add_u32 s27, s27, 0x3800\n"
"buffer_load_dwordx4 a[20:23], v4, s[12:15], s27 offen\n"
"s_add_u32 s27, s27, 0x3800\n"
"buffer_load_dwordx4 a[24:27], v4, s[12:15], s27 offen\n"
"s_add_u32 s27, s27, 0x3800\n"
"buffer_load_dwordx4 a[28:31], v4, s[12:15], s27 offen\n"
"s_add_u32 s27, s27, 0x3800\n"
"buffer_load_dwordx4 a[32:35], v4, s[12:15], s27 offen\n"
"s_add_u32 s27, s27, 0x3800\n"
"buffer_load_dwordx4 a[36:39], v4, s[12:15], s27 offen\n"
"s_add_u32 s27, s27, 0x3800\n"
"buffer_load_dwordx4 a[40:43], v4, s[12:15], s27 offen\n"
"s_add_u32 s27, s27, 0x3800\n"
"buffer_load_dwordx4 a[44:47], v4, s[12:15], s27 offen\n"
"s_add_u32 s27, s27, 0x3800\n"
"s_waitcnt vmcnt(24)\n"
"buffer_load_dwordx4 a[48:51], v4, s[12:15], s27 offen\n"
"s_add_u32 s27, s27, 0x3800\n"
"v_dot2_f32_bf16 v16, v128, s25, 0\n"
"v_dot2_f32_bf16 v17, v128, v128, 0\n"
"v_dot2_f32_bf16 v16, v129, s25, v16\n"
"v_dot2_f32_bf16 v17, v129, v129, v17\n"
"v_dot2_f32_bf16 v16, v130, s25, v16\n"
"v_dot2_f32_bf16 v17, v130, v130, v17\n"
"v_dot2_f32_bf16 v16, v131, s25, v16\n"
"v_dot2_f32_bf16 v17, v131, v131, v17\n"
"v_dot2_f32_bf16 v16, v132, s25, v16\n"
"buffer_load_dwordx4 a[52:55], v4, s[12:15], s27 offen\n"
"s_add_u32 s27, s27, 0x3800\n"
"v_dot2_f32_bf16 v17, v132, v132, v17\n"
"v_dot2_f32_bf16 v16, v133, s25, v16\n"
"v_dot2_f32_bf16 v17, v133, v133, v17\n"
"v_dot2_f32_bf16 v16, v134, s25, v16\n"
"v_dot2_f32_bf16 v17, v134, v134, v17\n"
"v_dot2_f32_bf16 v16, v135, s25, v16\n"
"v_dot2_f32_bf16 v17, v135, v135, v17\n"
"v_dot2_f32_bf16 v16, v136, s25, v16\n"
"v_dot2_f32_bf16 v17, v136, v136, v17\n"
"v_dot2_f32_bf16 v16, v137, s25, v16\n"
"buffer_load_dwordx4 a[56:59], v4, s[12:15], s27 offen\n"
"s_add_u32 s27, s27, 0x3800\n"
"v_dot2_f32_bf16 v17, v137, v137, v17\n"
"v_dot2_f32_bf16 v16, v138, s25, v16\n"
"v_dot2_f32_bf16 v17, v138, v138, v17\n"
"v_dot2_f32_bf16 v16, v139, s25, v16\n"
"v_dot2_f32_bf16 v17, v139, v139, v17\n"
"v_dot2_f32_bf16 v16, v140, s25, v16\n"
"v_dot2_f32_bf16 v17, v140, v140, v17\n"
"v_dot2_f32_bf16 v16, v141, s25, v16\n"
"v_dot2_f32_bf16 v17, v141, v141, v17\n"
"buffer_load_dwordx4 a[60:63], v4, s[12:15], s27 offen\n"
"s_add_u32 s27, s27, 0x3800\n"
"v_dot2_f32_bf16 v16, v142, s25, v16\n"
"v_dot2_f32_bf16 v17, v142, v142, v17\n"
"v_dot2_f32_bf16 v16, v143, s25, v16\n"
"v_dot2_f32_bf16 v17, v143, v143, v17\n"
"s_waitcnt vmcnt(24)\n"
"v_dot2_f32_bf16 v18, v144, s25, 0\n"
"v_dot2_f32_bf16 v19, v144, v144, 0\n"
"v_dot2_f32_bf16 v18, v145, s25, v18\n"
"v_dot2_f32_bf16 v19, v145, v145, v19\n"
"v_dot2_f32_bf16 v18, v146, s25, v18\n"
"buffer_load_dwordx4 a[64:67], v4, s[12:15], s27 offen\n"
"s_add_u32 s27, s27, 0x3800\n"
"v_dot2_f32_bf16 v19, v146, v146, v19\n"
"v_dot2_f32_bf16 v18, v147, s25, v18\n"
"v_dot2_f32_bf16 v19, v147, v147, v19\n"
"v_dot2_f32_bf16 v18, v148, s25, v18\n"
"v_dot2_f32_bf16 v19, v148, v148, v19\n"
"v_dot2_f32_bf16 v18, v149, s25, v18\n"
"v_dot2_f32_bf16 v19, v149, v149, v19\n"
"v_dot2_f32_bf16 v18, v150, s25, v18\n"
"v_dot2_f32_bf16 v19, v150, v150, v19\n"
"v_dot2_f32_bf16 v18, v151, s25, v18\n"
"buffer_load_dwordx4 a[68:71], v4, s[12:15], s27 offen\n"
"s_add_u32 s27, s27, 0x3800\n"
"v_dot2_f32_bf16 v19, v151, v151, v19\n"
"v_dot2_f32_bf16 v18, v152, s25, v18\n"
"v_dot2_f32_bf16 v19, v152, v152, v19\n"
"v_dot2_f32_bf16 v18, v153, s25, v18\n"
"v_dot2_f32_bf16 v19, v153, v153, v19\n"
"v_dot2_f32_bf16 v18, v154, s25, v18\n"
"v_dot2_f32_bf16 v19, v154, v154, v19\n"
"v_dot2_f32_bf16 v18, v155, s25, v18\n"
"v_dot2_f32_bf16 v19, v155, v155, v19\n"
"buffer_load_dwordx4 a[72:75], v4, s[12:15], s27 offen\n"
"s_add_u32 s27, s27, 0x3800\n"
"v_dot2_f32_bf16 v18, v156, s25, v18\n"
"v_dot2_f32_bf16 v19, v156, v156, v19\n"
"v_dot2_f32_bf16 v18, v157, s25, v18\n"
"v_dot2_f32_bf16 v19, v157, v157, v19\n"
"v_dot2_f32_bf16 v18, v158, s25, v18\n"
"v_dot2_f32_bf16 v19, v158, v158, v19\n"
"v_dot2_f32_bf16 v18, v159, s25, v18\n"
"v_dot2_f32_bf16 v19, v159, v159, v19\n"
"s_waitcnt vmcnt(23)\n"
"v_dot2_f32_bf16 v20, v160, s25, 0\n"
"buffer_load_dwordx4 a[76:79], v4, s[12:15], s27 offen\n"
"s_add_u32 s27, s27, 0x3800\n"
"v_dot2_f32_bf16 v21, v160, v160, 0\n"
"v_dot2_f32_bf16 v20, v161, s25, v20\n"
"v_dot2_f32_bf16 v21, v161, v161, v21\n"
"v_dot2_f32_bf16 v20, v162, s25, v20\n"
"v_dot2_f32_bf16 v21, v162, v162, v21\n"
"v_dot2_f32_bf16 v20, v163, s25, v20\n"
"v_dot2_f32_bf16 v21, v163, v163, v21\n"
"v_dot2_f32_bf16 v20, v164, s25, v20\n"
"v_dot2_f32_bf16 v21, v164, v164, v21\n"
"buffer_load_dwordx4 a[80:83], v4, s[12:15], s27 offen\n"
"s_add_u32 s27, s27, 0x3800\n"
"v_dot2_f32_bf16 v20, v165, s25, v20\n"
"v_dot2_f32_bf16 v21, v165, v165, v21\n"
"v_dot2_f32_bf16 v20, v166, s25, v20\n"
"v_dot2_f32_bf16 v21, v166, v166, v21\n"
"v_dot2_f32_bf16 v20, v167, s25, v20\n"
"v_dot2_f32_bf16 v21, v167, v167, v21\n"
"v_dot2_f32_bf16 v20, v168, s25, v20\n"
"v_dot2_f32_bf16 v21, v168, v168, v21\n"
"v_dot2_f32_bf16 v20, v169, s25, v20\n"
"v_dot2_f32_bf16 v21, v169, v169, v21\n"
"buffer_load_dwordx4 a[84:87], v4, s[12:15], s27 offen\n"
"s_add_u32 s27, s27, 0x3800\n"
"v_dot2_f32_bf16 v20, v170, s25, v20\n"
"v_dot2_f32_bf16 v21, v170, v170, v21\n"
"v_dot2_f32_bf16 v20, v171, s25, v20\n"
"v_dot2_f32_bf16 v21, v171, v171, v21\n"
"v_dot2_f32_bf16 v20, v172, s25, v20\n"
"v_dot2_f32_bf16 v21, v172, v172, v21\n"
"v_dot2_f32_bf16 v20, v173, s25, v20\n"
"v_dot2_f32_bf16 v21, v173, v173, v21\n"
"v_dot2_f32_bf16 v20, v174, s25, v20\n"
"v_dot2_f32_bf16 v21, v174, v174, v21\n"
"buffer_load_dwordx4 a[88:91], v4, s[12:15], s27 offen\n"
"s_add_u32 s27, s27, 0x3800\n"
"v_dot2_f32_bf16 v20, v175, s25, v20\n"
"v_dot2_f32_bf16 v21, v175, v175, v21\n"
"s_waitcnt vmcnt(23)\n"
"v_dot2_f32_bf16 v22, v176, s25, 0\n"
"v_dot2_f32_bf16 v23, v176, v176, 0\n"
"v_dot2_f32_bf16 v22, v177, s25, v22\n"
"v_dot2_f32_bf16 v23, v177, v177, v23\n"
"v_dot2_f32_bf16 v22, v178, s25, v22\n"
"v_dot2_f32_bf16 v23, v178, v178, v23\n"
"buffer_load_dwordx4 a[92:95], v4, s[12:15], s27 offen\n"
"s_add_u32 s27, s27, 0x3800\n"
"v_dot2_f32_bf16 v22, v179, s25, v22\n"
"v_dot2_f32_bf16 v23, v179, v179, v23\n"
"v_dot2_f32_bf16 v22, v180, s25, v22\n"
"v_dot2_f32_bf16 v23, v180, v180, v23\n"
"v_dot2_f32_bf16 v22, v181, s25, v22\n"
"v_dot2_f32_bf16 v23, v181, v181, v23\n"
"v_dot2_f32_bf16 v22, v182, s25, v22\n"
"v_dot2_f32_bf16 v23, v182, v182, v23\n"
"v_dot2_f32_bf16 v22, v183, s25, v22\n"
"v_dot2_f32_bf16 v23, v183, v183, v23\n"
"buffer_load_dwordx4 a[96:99], v4, s[12:15], s27 offen\n"
"s_add_u32 s27, s27, 0x3800\n"
"v_dot2_f32_bf16 v22, v184, s25, v22\n"
"v_dot2_f32_bf16 v23, v184, v184, v23\n"
"v_dot2_f32_bf16 v22, v185, s25, v22\n"
"v_dot2_f32_bf16 v23, v185, v185, v23\n"
"v_dot2_f32_bf16 v22, v186, s25, v22\n"
"v_dot2_f32_bf16 v23, v186, v186, v23\n"
"v_dot2_f32_bf16 v22, v187, s25, v22\n"
"v_dot2_f32_bf16 v23, v187, v187, v23\n"
"v_dot2_f32_bf16 v22, v188, s25, v22\n"
"buffer_load_dwordx4 a[100:103], v4, s[12:15], s27 offen\n"
"s_add_u32 s27, s27, 0x3800\n"
"v_dot2_f32_bf16 v23, v188, v188, v23\n"
"v_dot2_f32_bf16 v22, v189, s25, v22\n"
"v_dot2_f32_bf16 v23, v189, v189, v23\n"
"v_dot2_f32_bf16 v22, v190, s25, v22\n"
"v_dot2_f32_bf16 v23, v190, v190, v23\n"
"v_dot2_f32_bf16 v22, v191, s25, v22\n"
"v_dot2_f32_bf16 v23, v191, v191, v23\n"
"v_add_f32_dpp v16, v16, v16 row_ror:8 row_mask:0xf bank_mask:0xf\n"
"v_add_f32_dpp v17, v17, v17 row_ror:8 row_mask:0xf bank_mask:0xf\n"
"v_add_f32_dpp v18, v18, v18 row_ror:8 row_mask:0xf bank_mask:0xf\n"
"buffer_load_dwordx4 a[104:107], v4, s[12:15], s27 offen\n"
"s_add_u32 s27, s27, 0x3800\n"
"v_add_f32_dpp v19, v19, v19 row_ror:8 row_mask:0xf bank_mask:0xf\n"
"v_add_f32_dpp v20, v20, v20 row_ror:8 row_mask:0xf bank_mask:0xf\n"
"v_add_f32_dpp v21, v21, v21 row_ror:8 row_mask:0xf bank_mask:0xf\n"
"v_add_f32_dpp v22, v22, v22 row_ror:8 row_mask:0xf bank_mask:0xf\n"
"v_add_f32_dpp v23, v23, v23 row_ror:8 row_mask:0xf bank_mask:0xf\n"
"v_add_f32_dpp v16, v16, v16 row_ror:4 row_mask:0xf bank_mask:0xf\n"
"v_add_f32_dpp v17, v17, v17 row_ror:4 row_mask:0xf bank_mask:0xf\n"
"v_add_f32_dpp v18, v18, v18 row_ror:4 row_mask:0xf bank_mask:0xf\n"
"v_add_f32_dpp v19, v19, v19 row_ror:4 row_mask:0xf bank_mask:0xf\n"
"v_add_f32_dpp v20, v20, v20 row_ror:4 row_mask:0xf bank_mask:0xf\n"
"buffer_load_dwordx4 a[108:111], v4, s[12:15], s27 offen\n"
"s_add_u32 s27, s27, 0x3800\n"
"v_add_f32_dpp v21, v21, v21 row_ror:4 row_mask:0xf bank_mask:0xf\n"
"v_add_f32_dpp v22, v22, v22 row_ror:4 row_mask:0xf bank_mask:0xf\n"
"v_add_f32_dpp v23, v23, v23 row_ror:4 row_mask:0xf bank_mask:0xf\n"
"v_add_f32_dpp v16, v16, v16 row_ror:2 row_mask:0xf bank_mask:0xf\n"
"v_add_f32_dpp v17, v17, v17 row_ror:2 row_mask:0xf bank_mask:0xf\n"
"v_add_f32_dpp v18, v18, v18 row_ror:2 row_mask:0xf bank_mask:0xf\n"
"v_add_f32_dpp v19, v19, v19 row_ror:2 row_mask:0xf bank_mask:0xf\n"
"v_add_f32_dpp v20, v20, v20 row_ror:2 row_mask:0xf bank_mask:0xf\n"
"v_add_f32_dpp v21, v21, v21 row_ror:2 row_mask:0xf bank_mask:0xf\n"
"buffer_load_dwordx4 a[112:115], v4, s[12:15], s27 offen\n"
"s_add_u32 s27, s27, 0x3800\n"
"v_add_f32_dpp v22, v22, v22 row_ror:2 row_mask:0xf bank_mask:0xf\n"
"v_add_f32_dpp v23, v23, v23 row_ror:2 row_mask:0xf bank_mask:0xf\n"
"v_add_f32_dpp v16, v16, v16 row_ror:1 row_mask:0xf bank_mask:0xf\n"
"v_add_f32_dpp v17, v17, v17 row_ror:1 row_mask:0xf bank_mask:0xf\n"
"v_add_f32_dpp v18, v18, v18 row_ror:1 row_mask:0xf bank_mask:0xf\n"
"v_add_f32_dpp v19, v19, v19 row_ror:1 row_mask:0xf bank_mask:0xf\n"
"v_add_f32_dpp v20, v20, v20 row_ror:1 row_mask:0xf bank_mask:0xf\n"
"v_add_f32_dpp v21, v21, v21 row_ror:1 row_mask:0xf bank_mask:0xf\n"
"v_add_f32_dpp v22, v22, v22 row_ror:1 row_mask:0xf bank_mask:0xf\n"
"v_add_f32_dpp v23, v23, v23 row_ror:1 row_mask:0xf bank_mask:0xf\n"
"buffer_load_dwordx4 a[116:119], v4, s[12:15], s27 offen\n"
"s_add_u32 s27, s27, 0x3800\n"
"v_and_b32 v3, 3, v0\n"
"v_cmp_eq_u32 vcc, 1, v3\n"
"s_nop 1\n"
"v_cndmask_b32 v16, v16, v18, vcc\n"
"v_cndmask_b32 v17, v17, v19, vcc\n"
"v_cmp_eq_u32 vcc, 2, v3\n"
"s_nop 1\n"
"v_cndmask_b32 v16, v16, v20, vcc\n"
"v_cndmask_b32 v17, v17, v21, vcc\n"
"v_cmp_eq_u32 vcc, 3, v3\n"
"s_nop 1\n"
"v_cndmask_b32 v16, v16, v22, vcc\n"
"buffer_load_dwordx4 a[120:123], v4, s[12:15], s27 offen\n"
"s_add_u32 s27, s27, 0x3800\n"
"v_cndmask_b32 v17, v17, v23, vcc\n"
"v_mul_f32 v16, 0x3b000000, v16\n"
"v_mul_f32 v17, 0x3b000000, v17\n"
"v_fma_f32 v17, -v16, v16, v17\n"
"v_max_f32 v17, 0, v17\n"
"v_cvt_pk_bf16_f32 v18, v16, v17\n"
"v_lshlrev_b32 v16, 16, v18\n"
"v_and_b32 v17, 0xffff0000, v18\n"
"v_add_f32 v17, 0x3727c5ac, v17\n"
"v_cvt_pk_bf16_f32 v17, v17, v17\n"
"buffer_load_dwordx4 a[124:127], v4, s[12:15], s27 offen\n"
"s_add_u32 s27, s27, 0x3800\n"
"v_and_b32 v17, 0xffff0000, v17\n"
"v_sqrt_f32 v17, v17\n"
"s_nop 0\n"
"v_cvt_pk_bf16_f32 v17, v17, v17\n"
"v_and_b32 v17, 0xffff0000, v17\n"
"v_rcp_f32 v17, v17\n"
"v_lshl_add_u32 v18, v3, 2, v1\n"
"v_add_u32 v18, s53, v18\n"
"v_lshlrev_b32 v18, 3, v18\n"
"s_mov_b32 s24, 0x10000\n"
"s_mov_b32 exec_lo, 0x000f000f\n"
"s_mov_b32 exec_hi, 0x000f000f\n"
"buffer_store_dwordx2 v[16:17], v18, s[16:19], s24 offen sc1\n"
"s_mov_b64 exec, -1\n"
"s_waitcnt vmcnt(47)\n"
"v_readfirstlane_b32 s58, v32\n"
"v_readfirstlane_b32 s59, v33\n"
"s_mul_i32 s34, s58, 0x9e3779b1\n"
"s_add_u32 s34, s34, 0x7f4a7c15\n"
"s_xor_b32 s59, s59, s34\n"
"s_mul_i32 s35, s59, 0x85ebca77\n"
"s_add_u32 s35, s35, 0x165667b1\n"
"s_bfe_u32 s29, s6, 0x30003\n"
"s_lshl_b32 s28, s29, 9\n"
"v_lshl_add_u32 v53, v0, 3, s28\n"
"global_load_dwordx2 v[62:63], v53, s[56:57]\n"
"s_and_b32 s56, s6, 7\n"
"s_lshl_b32 s56, s56, 2\n"
"s_lshr_b32 s28, s6, 6\n"
"s_add_u32 s56, s56, s28\n"
"s_bfe_u32 s57, s6, 0x30003\n"
"s_lshl_b32 s28, s56, 16\n"
"s_lshl_b32 s29, s7, 14\n"
"s_add_u32 s28, s28, s29\n"
"s_lshl_b32 s29, s57, 7\n"
"s_add_u32 s28, s28, s29\n"
"v_lshrrev_b32 v51, 3, v0\n"
"v_lshlrev_b32 v51, 11, v51\n"
"v_and_b32 v53, 7, v0\n"
"v_lshl_add_u32 v51, v53, 4, v51\n"
"v_add_u32 v51, s28, v51\n"
"v_subrev_u32 v53, 0xc00, v51\n"
"buffer_load_dwordx4 v[64:67], v53, s[92:95], 0 offen\n"
"v_subrev_u32 v53, 0x800, v51\n"
"buffer_load_dwordx4 v[68:71], v53, s[92:95], 0 offen\n"
"v_subrev_u32 v53, 0x400, v51\n"
"buffer_load_dwordx4 v[72:75], v53, s[92:95], 0 offen\n"
"buffer_load_dwordx4 v[76:79], v51, s[92:95], 0 offen offset:0\n"
"buffer_load_dwordx4 v[80:83], v51, s[92:95], 0 offen offset:1024\n"
"buffer_load_dwordx4 v[84:87], v51, s[96:99], 0 offen offset:0\n"
"buffer_load_dwordx4 v[88:91], v51, s[96:99], 0 offen offset:1024\n"
"s_waitcnt vmcnt(37)\n"
"v_mfma_f32_16x16x32_bf16 v[36:39], v[44:47], a[0:3], 0\n"
"v_mfma_f32_16x16x32_bf16 v[40:43], v[44:47], a[4:7], 0\n"
"v_mfma_f32_16x16x32_bf16 v[36:39], v[44:47], a[8:11], v[36:39]\n"
"v_mfma_f32_16x16x32_bf16 v[40:43], v[44:47], a[12:15], v[40:43]\n"
"s_waitcnt vmcnt(33)\n"
"v_mfma_f32_16x16x32_bf16 v[36:39], v[44:47], a[16:19], v[36:39]\n"
"v_mfma_f32_16x16x32_bf16 v[40:43], v[44:47], a[20:23], v[40:43]\n"
"v_mfma_f32_16x16x32_bf16 v[36:39], v[44:47], a[24:27], v[36:39]\n"
"v_mfma_f32_16x16x32_bf16 v[40:43], v[44:47], a[28:31], v[40:43]\n"
"s_waitcnt vmcnt(29)\n"
"v_mfma_f32_16x16x32_bf16 v[36:39], v[44:47], a[32:35], v[36:39]\n"
"v_mfma_f32_16x16x32_bf16 v[40:43], v[44:47], a[36:39], v[40:43]\n"
"v_mfma_f32_16x16x32_bf16 v[36:39], v[44:47], a[40:43], v[36:39]\n"
"v_mfma_f32_16x16x32_bf16 v[40:43], v[44:47], a[44:47], v[40:43]\n"
"s_waitcnt vmcnt(25)\n"
"v_mfma_f32_16x16x32_bf16 v[36:39], v[44:47], a[48:51], v[36:39]\n"
"v_mfma_f32_16x16x32_bf16 v[40:43], v[44:47], a[52:55], v[40:43]\n"
"v_mfma_f32_16x16x32_bf16 v[36:39], v[44:47], a[56:59], v[36:39]\n"
"v_mfma_f32_16x16x32_bf16 v[40:43], v[44:47], a[60:63], v[40:43]\n"
"s_waitcnt vmcnt(21)\n"
"v_mfma_f32_16x16x32_bf16 v[36:39], v[44:47], a[64:67], v[36:39]\n"
"v_mfma_f32_16x16x32_bf16 v[40:43], v[44:47], a[68:71], v[40:43]\n"
"v_mfma_f32_16x16x32_bf16 v[36:39], v[44:47], a[72:75], v[36:39]\n"
"v_mfma_f32_16x16x32_bf16 v[40:43], v[44:47], a[76:79], v[40:43]\n"
"s_waitcnt vmcnt(17)\n"
"v_mfma_f32_16x16x32_bf16 v[36:39], v[44:47], a[80:83], v[36:39]\n"
"v_mfma_f32_16x16x32_bf16 v[40:43], v[44:47], a[84:87], v[40:43]\n"
"v_mfma_f32_16x16x32_bf16 v[36:39], v[44:47], a[88:91], v[36:39]\n"
"v_mfma_f32_16x16x32_bf16 v[40:43], v[44:47], a[92:95], v[40:43]\n"
"s_waitcnt vmcnt(13)\n"
"v_mfma_f32_16x16x32_bf16 v[36:39], v[44:47], a[96:99], v[36:39]\n"
"v_mfma_f32_16x16x32_bf16 v[40:43], v[44:47], a[100:103], v[40:43]\n"
"v_mfma_f32_16x16x32_bf16 v[36:39], v[44:47], a[104:107], v[36:39]\n"
"v_mfma_f32_16x16x32_bf16 v[40:43], v[44:47], a[108:111], v[40:43]\n"
"s_waitcnt vmcnt(9)\n"
"v_mfma_f32_16x16x32_bf16 v[36:39], v[44:47], a[112:115], v[36:39]\n"
"v_mfma_f32_16x16x32_bf16 v[40:43], v[44:47], a[116:119], v[40:43]\n"
"v_mfma_f32_16x16x32_bf16 v[36:39], v[44:47], a[120:123], v[36:39]\n"
"v_mfma_f32_16x16x32_bf16 v[40:43], v[44:47], a[124:127], v[40:43]\n"
"s_lshl_b32 s24, s7, 9\n"
"v_lshl_add_u32 v6, v2, 5, s24\n"
"v_lshl_add_u32 v6, v1, 4, v6\n"
"s_and_b32 s24, s7, 1\n"
"s_lshl_b32 s24, s24, 8\n"
"v_lshl_add_u32 v3, v0, 2, s24\n"
"s_lshl_b32 s27, s52, 6\n"
"s_lshl_b32 s29, s7, 3\n"
"s_add_u32 s27, s27, s29\n"
"s_add_u32 s27, s27, 0x1000\n"
"s_lshl_b32 s24, s20, 9\n"
"s_add_u32 s24, s24, 0x14000\n"
"s_lshl_b32 s20, s20, 6\n"
"s_add_u32 s20, s20, s29\n"
"s_add_u32 s20, s20, 0x2000\n"
"v_pk_add_f32 v[36:37], v[36:37], v[40:41]\n"
"v_pk_add_f32 v[38:39], v[38:39], v[42:43]\n"
"s_mov_b32 exec_hi, 0\n"
"ds_write_b128 v6, v[36:39]\n"
"s_mov_b32 exec_hi, -1\n"
"s_waitcnt vmcnt(8)\n"
"v_mov_b32 v8, s34\n"
"v_mov_b32 v9, s35\n"
"v_mov_b32 v10, s27\n"
"s_mov_b64 exec, 1\n"
"buffer_store_dwordx2 v[8:9], v10, s[16:19], 0 offen sc1\n"
"s_mov_b64 exec, -1\n"
"s_waitcnt lgkmcnt(0)\n"
"s_barrier\n"
"ds_read_b32 v40, v3 offset:0\n"
"ds_read_b32 v41, v3 offset:512\n"
"ds_read_b32 v42, v3 offset:1024\n"
"ds_read_b32 v43, v3 offset:1536\n"
"s_waitcnt lgkmcnt(0)\n"
"v_add_f32 v40, v40, v41\n"
"v_add_f32 v42, v42, v43\n"
"v_add_f32 v40, v40, v42\n"
"s_cmp_lt_u32 s7, 2\n"
"s_cselect_b32 s23, s23, 0\n"
"s_cmp_eq_u32 s23, 0\n"
"s_cbranch_scc1 .L_nocs%=\n"
"buffer_store_dword v40, v3, s[16:19], s24 offen sc1\n"
"s_waitcnt vmcnt(0)\n"
"v_mov_b32 v8, s34\n"
"v_mov_b32 v9, s35\n"
"v_mov_b32 v10, s20\n"
"s_mov_b64 exec, 1\n"
"buffer_store_dwordx2 v[8:9], v10, s[16:19], 0 offen sc1\n"
"s_mov_b64 exec, -1\n"
".L_nocs%=:\n"
"s_lshl_b32 s22, s6, 13\n"
"s_lshl_b32 s23, s7, 10\n"
"s_add_u32 s23, s23, s22\n"
"v_lshl_add_u32 v5, v0, 4, s23\n"
"s_movk_i32 s55, 0x1000\n"
"buffer_load_dwordx4 v[36:39], v5, s[36:39], 0 offen\n"
"buffer_load_dwordx2 v[40:41], v5, s[36:39], 0 offen offset:16\n"
"buffer_load_dwordx4 v[42:45], v5, s[36:39], s55 offen\n"
"buffer_load_dwordx2 v[46:47], v5, s[36:39], s55 offen offset:16\n"
"s_lshr_b32 s23, s7, 1\n"
"s_lshl_b32 s23, s23, 12\n"
"s_add_u32 s23, s23, s22\n"
"v_mov_b32 v28, s23\n"
"buffer_load_dwordx2 v[48:49], v28, s[36:39], 0 offen\n"
"s_lshl_b32 s23, s6, 2\n"
"s_and_b32 s55, s7, 2\n"
"s_add_u32 s23, s23, s55\n"
"v_mov_b32 v28, s23\n"
"global_load_ushort v50, v28, s[50:51]\n"
"s_lshl_b32 s28, s7, 9\n"
"s_add_u32 s28, s28, 153600\n"
"v_lshl_add_u32 v53, v0, 3, s28\n"
"s_waitcnt vmcnt(14)\n"
"ds_write_b64 v53, v[62:63]\n"
"v_and_b32 v53, 7, v0\n"
"v_lshl_add_u32 v53, v53, 6, s28\n"
"s_waitcnt lgkmcnt(0)\n"
"ds_read_b128 v[92:95], v53 offset:0\n"
"ds_read_b128 v[96:99], v53 offset:16\n"
"ds_read_b128 v[100:103], v53 offset:32\n"
"ds_read_b128 v[104:107], v53 offset:48\n"
"s_mov_b32 s58, 0x05040100\n"
"s_mov_b32 s59, 0x07060302\n"
"s_waitcnt vmcnt(7)\n"
"s_waitcnt lgkmcnt(0)\n"
"v_perm_b32 v108, v72, v76, s58\n"
"v_perm_b32 v109, v72, v76, s59\n"
"v_perm_b32 v110, v73, v77, s58\n"
"v_perm_b32 v111, v73, v77, s59\n"
"v_perm_b32 v112, v74, v78, s58\n"
"v_perm_b32 v113, v74, v78, s59\n"
"v_perm_b32 v114, v75, v79, s58\n"
"v_perm_b32 v115, v75, v79, s59\n"
"v_perm_b32 v116, v64, v68, s58\n"
"v_perm_b32 v117, v64, v68, s59\n"
"v_perm_b32 v118, v65, v69, s58\n"
"v_perm_b32 v119, v65, v69, s59\n"
"v_perm_b32 v120, v66, v70, s58\n"
"v_perm_b32 v121, v66, v70, s59\n"
"v_perm_b32 v122, v67, v71, s58\n"
"v_perm_b32 v123, v67, v71, s59\n"
"v_dot2_f32_bf16 v54, v108, v92, 0\n"
"v_dot2_f32_bf16 v55, v109, v94, 0\n"
"v_dot2_f32_bf16 v56, v110, v96, 0\n"
"v_dot2_f32_bf16 v57, v111, v98, 0\n"
"v_dot2_f32_bf16 v58, v112, v100, 0\n"
"v_dot2_f32_bf16 v59, v113, v102, 0\n"
"v_dot2_f32_bf16 v60, v114, v104, 0\n"
"v_dot2_f32_bf16 v61, v115, v106, 0\n"
"v_dot2c_f32_bf16 v54, v116, v93\n"
"v_dot2c_f32_bf16 v55, v117, v95\n"
"v_dot2c_f32_bf16 v56, v118, v97\n"
"v_dot2c_f32_bf16 v57, v119, v99\n"
"v_dot2c_f32_bf16 v58, v120, v101\n"
"v_dot2c_f32_bf16 v59, v121, v103\n"
"v_dot2c_f32_bf16 v60, v122, v105\n"
"v_dot2c_f32_bf16 v61, v123, v107\n"
"v_lshlrev_b32 v108, 16, v84\n"
"v_and_b32 v109, 0xffff0000, v84\n"
"v_lshlrev_b32 v110, 16, v85\n"
"v_and_b32 v111, 0xffff0000, v85\n"
"v_lshlrev_b32 v112, 16, v86\n"
"v_and_b32 v113, 0xffff0000, v86\n"
"v_lshlrev_b32 v114, 16, v87\n"
"v_and_b32 v115, 0xffff0000, v87\n"
"v_mul_f32 v54, v54, v108\n"
"v_mul_f32 v55, v55, v109\n"
"v_mul_f32 v56, v56, v110\n"
"v_mul_f32 v57, v57, v111\n"
"v_mul_f32 v58, v58, v112\n"
"v_mul_f32 v59, v59, v113\n"
"v_mul_f32 v60, v60, v114\n"
"v_mul_f32 v61, v61, v115\n"
"v_lshrrev_b32 v51, 3, v0\n"
"s_lshl_b32 s28, s7, 3\n"
"s_sub_u32 s28, 31, s28\n"
"v_sub_u32 v51, s28, v51\n"
"v_and_b32 v53, 7, v0\n"
"v_lshlrev_b32 v62, 2, v53\n"
"v_xor_b32 v51, v51, v62\n"
"v_lshlrev_b32 v51, 2, v51\n"
"v_lshl_add_u32 v51, v53, 10, v51\n"
"v_add_u32 v51, 155648, v51\n"
"v_perm_b32 v108, v76, v80, s58\n"
"v_perm_b32 v109, v76, v80, s59\n"
"v_perm_b32 v110, v77, v81, s58\n"
"v_perm_b32 v111, v77, v81, s59\n"
"v_perm_b32 v112, v78, v82, s58\n"
"v_perm_b32 v113, v78, v82, s59\n"
"v_perm_b32 v114, v79, v83, s58\n"
"v_perm_b32 v115, v79, v83, s59\n"
"v_perm_b32 v116, v68, v72, s58\n"
"v_perm_b32 v117, v68, v72, s59\n"
"v_perm_b32 v118, v69, v73, s58\n"
"v_perm_b32 v119, v69, v73, s59\n"
"v_perm_b32 v120, v70, v74, s58\n"
"v_perm_b32 v121, v70, v74, s59\n"
"v_perm_b32 v122, v71, v75, s58\n"
"v_perm_b32 v123, v71, v75, s59\n"
"v_dot2_f32_bf16 v64, v108, v92, 0\n"
"v_dot2_f32_bf16 v65, v109, v94, 0\n"
"v_dot2_f32_bf16 v66, v110, v96, 0\n"
"v_dot2_f32_bf16 v67, v111, v98, 0\n"
"v_dot2_f32_bf16 v68, v112, v100, 0\n"
"v_dot2_f32_bf16 v69, v113, v102, 0\n"
"v_dot2_f32_bf16 v70, v114, v104, 0\n"
"v_dot2_f32_bf16 v71, v115, v106, 0\n"
"v_dot2c_f32_bf16 v64, v116, v93\n"
"v_dot2c_f32_bf16 v65, v117, v95\n"
"v_dot2c_f32_bf16 v66, v118, v97\n"
"v_dot2c_f32_bf16 v67, v119, v99\n"
"v_dot2c_f32_bf16 v68, v120, v101\n"
"v_dot2c_f32_bf16 v69, v121, v103\n"
"v_dot2c_f32_bf16 v70, v122, v105\n"
"v_dot2c_f32_bf16 v71, v123, v107\n"
"v_lshlrev_b32 v108, 16, v88\n"
"v_and_b32 v109, 0xffff0000, v88\n"
"v_lshlrev_b32 v110, 16, v89\n"
"v_and_b32 v111, 0xffff0000, v89\n"
"v_lshlrev_b32 v112, 16, v90\n"
"v_and_b32 v113, 0xffff0000, v90\n"
"v_lshlrev_b32 v114, 16, v91\n"
"v_and_b32 v115, 0xffff0000, v91\n"
"v_mul_f32 v64, v64, v108\n"
"v_mul_f32 v65, v65, v109\n"
"v_mul_f32 v66, v66, v110\n"
"v_mul_f32 v67, v67, v111\n"
"v_mul_f32 v68, v68, v112\n"
"v_mul_f32 v69, v69, v113\n"
"v_mul_f32 v70, v70, v114\n"
"v_mul_f32 v71, v71, v115\n"
"v_cvt_pk_bf16_f32 v108, v64, v54\n"
"ds_write_b32 v51, v108 offset:0\n"
"v_cvt_pk_bf16_f32 v109, v65, v55\n"
"ds_write_b32 v51, v109 offset:128\n"
"v_cvt_pk_bf16_f32 v110, v66, v56\n"
"ds_write_b32 v51, v110 offset:256\n"
"v_cvt_pk_bf16_f32 v111, v67, v57\n"
"ds_write_b32 v51, v111 offset:384\n"
"v_cvt_pk_bf16_f32 v112, v68, v58\n"
"ds_write_b32 v51, v112 offset:512\n"
"v_cvt_pk_bf16_f32 v113, v69, v59\n"
"ds_write_b32 v51, v113 offset:640\n"
"v_cvt_pk_bf16_f32 v114, v70, v60\n"
"ds_write_b32 v51, v114 offset:768\n"
"v_cvt_pk_bf16_f32 v115, v71, v61\n"
"ds_write_b32 v51, v115 offset:896\n"
"s_waitcnt lgkmcnt(0)\n"
"s_barrier\n"
"s_lshl_b32 s28, s7, 1\n"
"v_and_b32 v53, 7, v0\n"
"v_xor_b32 v51, s28, v53\n"
"v_lshlrev_b32 v51, 4, v51\n"
"v_lshrrev_b32 v62, 3, v0\n"
"v_lshl_add_u32 v51, v62, 7, v51\n"
"s_lshl_b32 s28, s7, 11\n"
"s_add_u32 s28, s28, 155648\n"
"v_add_u32 v51, s28, v51\n"
"ds_read_b128 v[54:57], v51\n"
"v_xor_b32 v51, 16, v51\n"
"ds_read_b128 v[58:61], v51 offset:1024\n"
"v_lshlrev_b32 v53, 4, v0\n"
"s_lshl_b32 s28, s57, 13\n"
"s_lshl_b32 s29, s7, 11\n"
"s_add_u32 s28, s28, s29\n"
"s_mul_i32 s29, s56, 65792\n"
"s_sub_u32 s28, s28, s29\n"
"s_add_u32 s58, s28, 0x2f1f00\n"
"s_waitcnt lgkmcnt(0)\n"
"buffer_store_dwordx4 v[54:57], v53, s[16:19], s58 offen sc1\n"
"buffer_store_dwordx4 v[58:61], v53, s[16:19], s58 offen offset:1024 sc1\n"
"s_barrier\n"
"s_mul_i32 s52, s7, 448\n"
"v_lshrrev_b32 v3, 3, v0\n"
"v_add_u32 v3, s52, v3\n"
"v_lshlrev_b32 v3, 10, v3\n"
"s_lshl_b32 s52, s49, 7\n"
"v_add_u32 v3, s52, v3\n"
"v_lshlrev_b32 v4, 1, v1\n"
"v_and_b32 v5, 7, v0\n"
"v_xor_b32 v4, v4, v5\n"
"v_lshl_add_u32 v30, v4, 4, v3\n"
"v_add_u32 v31, 0x1c00, v30\n"
"v_add_u32 v32, 0x3800, v30\n"
"v_add_u32 v33, 0x5400, v30\n"
"v_bfe_u32 v3, v0, 2, 2\n"
"v_lshl_add_u32 v3, v1, 2, v3\n"
"v_bfe_u32 v4, v3, 1, 2\n"
"v_lshlrev_b32 v3, 7, v3\n"
"v_lshl_add_u32 v3, v4, 5, v3\n"
"v_and_b32 v4, 3, v0\n"
"v_lshl_add_u32 v29, v4, 3, v3\n"
"v_add_u32 v8, s26, v29\n"
"v_xor_b32 v9, 32, v8\n"
"v_xor_b32 v10, 64, v8\n"
"v_xor_b32 v11, 0x60, v8\n"
"v_add_u32 v12, 0x10000, v8\n"
"v_add_u32 v13, 0x10000, v9\n"
"v_add_u32 v14, 0x10000, v10\n"
"v_add_u32 v15, 0x10000, v11\n"
"s_lshl_b32 s28, s57, 11\n"
"s_lshl_b32 s29, s56, 6\n"
"s_add_u32 s28, s28, s29\n"
"s_lshl_b32 s29, s7, 3\n"
"s_add_u32 s28, s28, s29\n"
"s_add_u32 s28, s28, 0xc000\n"
"s_waitcnt vmcnt(0)\n"
"v_mov_b32 v120, s34\n"
"v_mov_b32 v121, s35\n"
"v_mov_b32 v122, s28\n"
"s_mov_b64 exec, 1\n"
"buffer_store_dwordx2 v[120:121], v122, s[16:19], 0 offen sc1\n"
"s_mov_b64 exec, -1\n"
"s_add_u32 m0, s26, 0x14100\n"
"s_add_u32 s24, s86, 0x0\n"
"s_cmp_ge_u32 s24, 0x70000\n"
"s_cselect_b32 s31, 0x70000, 0\n"
"s_sub_u32 s24, s24, s31\n"
"buffer_load_dwordx4 v30, s[40:43], s24 offen offset:0 lds\n"
"buffer_load_dwordx4 v31, s[40:43], s24 offen offset:1024 lds\n"
"buffer_load_dwordx4 v32, s[40:43], s24 offen offset:2048 lds\n"
"buffer_load_dwordx4 v33, s[40:43], s24 offen offset:3072 lds\n"
"s_add_u32 m0, s26, 0x4100\n"
"s_add_u32 s24, s86, 0x10000\n"
"s_cmp_ge_u32 s24, 0x70000\n"
"s_cselect_b32 s31, 0x70000, 0\n"
"s_sub_u32 s24, s24, s31\n"
"buffer_load_dwordx4 v30, s[40:43], s24 offen offset:0 lds\n"
"buffer_load_dwordx4 v31, s[40:43], s24 offen offset:1024 lds\n"
"buffer_load_dwordx4 v32, s[40:43], s24 offen offset:2048 lds\n"
"buffer_load_dwordx4 v33, s[40:43], s24 offen offset:3072 lds\n"
"s_add_u32 m0, s26, 0x8100\n"
"s_add_u32 s24, s86, 0x20000\n"
"s_cmp_ge_u32 s24, 0x70000\n"
"s_cselect_b32 s31, 0x70000, 0\n"
"s_sub_u32 s24, s24, s31\n"
"buffer_load_dwordx4 v30, s[40:43], s24 offen offset:0 lds\n"
"buffer_load_dwordx4 v31, s[40:43], s24 offen offset:1024 lds\n"
"buffer_load_dwordx4 v32, s[40:43], s24 offen offset:2048 lds\n"
"buffer_load_dwordx4 v33, s[40:43], s24 offen offset:3072 lds\n"
"s_add_u32 m0, s26, 0x18100\n"
"s_add_u32 s24, s86, 0x30000\n"
"s_cmp_ge_u32 s24, 0x70000\n"
"s_cselect_b32 s31, 0x70000, 0\n"
"s_sub_u32 s24, s24, s31\n"
"buffer_load_dwordx4 v30, s[40:43], s24 offen offset:0 lds\n"
"buffer_load_dwordx4 v31, s[40:43], s24 offen offset:1024 lds\n"
"buffer_load_dwordx4 v32, s[40:43], s24 offen offset:2048 lds\n"
"buffer_load_dwordx4 v33, s[40:43], s24 offen offset:3072 lds\n"
"s_add_u32 m0, s26, 0xc100\n"
"s_add_u32 s24, s86, 0x40000\n"
"s_cmp_ge_u32 s24, 0x70000\n"
"s_cselect_b32 s31, 0x70000, 0\n"
"s_sub_u32 s24, s24, s31\n"
"buffer_load_dwordx4 v30, s[40:43], s24 offen offset:0 lds\n"
"buffer_load_dwordx4 v31, s[40:43], s24 offen offset:1024 lds\n"
"buffer_load_dwordx4 v32, s[40:43], s24 offen offset:2048 lds\n"
"buffer_load_dwordx4 v33, s[40:43], s24 offen offset:3072 lds\n"
"s_add_u32 m0, s26, 0x10100\n"
"s_add_u32 s24, s86, 0x50000\n"
"s_cmp_ge_u32 s24, 0x70000\n"
"s_cselect_b32 s31, 0x70000, 0\n"
"s_sub_u32 s24, s24, s31\n"
"buffer_load_dwordx4 v30, s[40:43], s24 offen offset:0 lds\n"
"buffer_load_dwordx4 v31, s[40:43], s24 offen offset:1024 lds\n"
"buffer_load_dwordx4 v32, s[40:43], s24 offen offset:2048 lds\n"
"buffer_load_dwordx4 v33, s[40:43], s24 offen offset:3072 lds\n"
"s_add_u32 m0, s26, 0x1c100\n"
"s_add_u32 s24, s86, 0x60000\n"
"s_cmp_ge_u32 s24, 0x70000\n"
"s_cselect_b32 s31, 0x70000, 0\n"
"s_sub_u32 s24, s24, s31\n"
"buffer_load_dwordx4 v30, s[40:43], s24 offen offset:0 lds\n"
"buffer_load_dwordx4 v31, s[40:43], s24 offen offset:1024 lds\n"
"buffer_load_dwordx4 v32, s[40:43], s24 offen offset:2048 lds\n"
"buffer_load_dwordx4 v33, s[40:43], s24 offen offset:3072 lds\n"
"s_lshr_b32 s52, s6, 5\n"
"s_lshl_b32 s52, s52, 11\n"
"s_bfe_u32 s53, s6, 0x20003\n"
"s_lshl_b32 s53, s53, 3\n"
"s_add_u32 s52, s52, s53\n"
"s_add_u32 s52, s52, 0xc000\n"
"v_and_b32 v126, 31, v0\n"
"v_lshl_add_u32 v126, v126, 6, s52\n"
"s_add_u32 s5, s7, s49\n"
"s_and_b32 s5, s5, 3\n"
"s_lshl_b32 s52, s48, 6\n"
"s_lshl_b32 s53, s5, 3\n"
"s_add_u32 s52, s52, s53\n"
"s_add_u32 s52, s52, 0x1000\n"
"v_mov_b32 v4, s52\n"
"v_cmp_lt_u32 vcc, 31, v0\n"
"s_nop 1\n"
"v_cndmask_b32 v126, v126, v4, vcc\n"
"s_lshr_b32 s52, s7, 1\n"
"s_mul_i32 s30, s52, 131328\n"
"s_mul_i32 s52, s52, 4608\n"
"s_add_u32 s87, s52, 147968\n"
"s_and_b32 s53, s7, 1\n"
"s_lshl_b32 s53, s53, 8\n"
"s_add_u32 s52, s30, s53\n"
"v_and_b32 v34, 3, v0\n"
"v_mul_u32_u24 v124, 4160, v34\n"
"v_lshl_add_u32 v34, v1, 4, v124\n"
"v_bfe_u32 v4, v0, 2, 2\n"
"v_lshl_add_u32 v34, v4, 3, v34\n"
"v_add_u32 v34, s52, v34\n"
"s_add_u32 s53, s53, s87\n"
"s_add_u32 s53, s53, 480\n"
"v_lshlrev_b32 v35, 5, v2\n"
"v_sub_u32 v35, s53, v35\n"
"v_lshl_add_u32 v35, v1, 4, v35\n"
"v_bfe_u32 v4, v0, 2, 1\n"
"v_lshl_add_u32 v124, v4, 4, v124\n"
"v_add_u32 v124, s30, v124\n"
"s_lshl_b32 s52, s7, 10\n"
"s_add_u32 s52, s52, 32\n"
"v_lshl_add_u32 v125, v0, 4, s52\n"
"v_lshlrev_b32 v52, 4, v0\n"
"s_waitcnt vmcnt(31)\n"
"v_alignbit_b32 v54, v37, v36, 16\n"
"v_alignbit_b32 v55, v38, v37, 16\n"
"v_alignbit_b32 v56, v39, v38, 16\n"
"v_alignbit_b32 v57, v40, v39, 16\n"
"v_mov_b32 v104, v37\n"
"v_mov_b32 v105, v38\n"
"v_mov_b32 v106, v39\n"
"v_mov_b32 v107, v40\n"
"v_alignbit_b32 v58, v38, v37, 16\n"
"v_alignbit_b32 v59, v39, v38, 16\n"
"v_alignbit_b32 v60, v40, v39, 16\n"
"v_alignbit_b32 v61, v41, v40, 16\n"
"v_alignbit_b32 v108, v43, v42, 16\n"
"v_alignbit_b32 v109, v44, v43, 16\n"
"v_alignbit_b32 v110, v45, v44, 16\n"
"v_alignbit_b32 v111, v46, v45, 16\n"
"v_mov_b32 v116, v43\n"
"v_mov_b32 v117, v44\n"
"v_mov_b32 v118, v45\n"
"v_mov_b32 v119, v46\n"
"v_alignbit_b32 v112, v44, v43, 16\n"
"v_alignbit_b32 v113, v45, v44, 16\n"
"v_alignbit_b32 v114, v46, v45, 16\n"
"v_alignbit_b32 v115, v47, v46, 16\n"
"v_alignbit_b32 v53, v49, v48, 16\n"
"v_add_u32 v51, 0x20100, v125\n"
"v_and_b32 v62, 31, v0\n"
"v_lshl_add_u32 v62, v62, 4, s87\n"
"v_mov_b32 v63, s30\n"
"v_sub_u32 v28, 15, v2\n"
"v_lshlrev_b32 v5, 3, v1\n"
"v_sub_u32 v28, v28, v5\n"
"s_and_b32 s22, s7, 1\n"
"s_lshl_b32 s22, s22, 6\n"
"v_or_b32 v28, s22, v28\n"
"v_lshlrev_b32 v5, 16, v50\n"
"v_cmp_eq_u32 s[58:59], 0, v28\n"
"v_cmp_eq_u32 s[56:57], 1, v28\n"
"v_accvgpr_write_b32 a208, 0\n"
"v_cndmask_b32 v24, 0, v50, s[58:59]\n"
"s_nop 0\n"
"v_cndmask_b32 v24, v24, v5, s[56:57]\n"
"v_cmp_eq_u32 s[58:59], 2, v28\n"
"v_cmp_eq_u32 s[56:57], 3, v28\n"
"v_accvgpr_write_b32 a209, 0\n"
"v_cndmask_b32 v25, 0, v50, s[58:59]\n"
"s_nop 0\n"
"v_cndmask_b32 v25, v25, v5, s[56:57]\n"
"v_cmp_eq_u32 s[58:59], 4, v28\n"
"v_cmp_eq_u32 s[56:57], 5, v28\n"
"v_accvgpr_write_b32 a210, 0\n"
"v_cndmask_b32 v26, 0, v50, s[58:59]\n"
"s_nop 0\n"
"v_cndmask_b32 v26, v26, v5, s[56:57]\n"
"v_cmp_eq_u32 s[58:59], 6, v28\n"
"v_cmp_eq_u32 s[56:57], 7, v28\n"
"v_accvgpr_write_b32 a211, 0\n"
"v_cndmask_b32 v27, 0, v50, s[58:59]\n"
"s_nop 0\n"
"v_cndmask_b32 v27, v27, v5, s[56:57]\n"
"ds_write_b128 v51, v[108:111] offset:0\n"
"ds_write_b128 v51, v[116:119] offset:4160\n"
"ds_write_b128 v51, v[112:115] offset:8320\n"
"ds_write_b128 v51, v[44:47] offset:12480\n"
"ds_write_b128 v125, v[54:57] offset:0\n"
"ds_write_b128 v125, v[104:107] offset:4160\n"
"ds_write_b128 v125, v[58:61] offset:8320\n"
"ds_write_b128 v125, v[38:41] offset:12480\n"
"ds_write_b128 v62, a[208:211] offset:4096\n"
"s_mov_b64 exec, 0xff\n"
"ds_write_b128 v124, a[208:211]\n"
"s_mov_b64 exec, 1\n"
"ds_write_b16 v63, v48 offset:30\n"
"ds_write_b32 v63, v48 offset:4188\n"
"ds_write_b16 v63, v48 offset:8346\n"
"ds_write_b32 v63, v53 offset:8348\n"
"ds_write_b64 v63, v[48:49] offset:12504\n"
"s_mov_b64 exec, -1\n"
"s_lshl_b32 s28, s6, 2\n"
"s_add_u32 s28, s28, s7\n"
"s_cmp_lg_u32 s28, 896\n"
"s_cbranch_scc1 .L_nouf%=\n"
"v_lshlrev_b32 v3, 6, v0\n"
"v_add_u32 v3, 0xc000, v3\n"
"buffer_load_dwordx2 v[4:5], v3, s[16:19], 0 offen sc1\n"
"s_waitcnt vmcnt(0)\n"
"v_cmp_ne_u64 vcc, s[34:35], v[4:5]\n"
"s_nop 4\n"
"s_cbranch_vccnz .L_slow_go0%=\n"
".L_back_go0%=:\n"
"v_lshlrev_b32 v3, 6, v0\n"
"v_add_u32 v3, 0xd000, v3\n"
"buffer_load_dwordx2 v[4:5], v3, s[16:19], 0 offen sc1\n"
"s_waitcnt vmcnt(0)\n"
"v_cmp_ne_u64 vcc, s[34:35], v[4:5]\n"
"s_nop 4\n"
"s_cbranch_vccnz .L_slow_go1%=\n"
".L_back_go1%=:\n"
"v_lshlrev_b32 v3, 6, v0\n"
"v_add_u32 v3, 0xe000, v3\n"
"buffer_load_dwordx2 v[4:5], v3, s[16:19], 0 offen sc1\n"
"s_waitcnt vmcnt(0)\n"
"v_cmp_ne_u64 vcc, s[34:35], v[4:5]\n"
"s_nop 4\n"
"s_cbranch_vccnz .L_slow_go2%=\n"
".L_back_go2%=:\n"
"v_lshlrev_b32 v3, 6, v0\n"
"v_add_u32 v3, 0xf000, v3\n"
"buffer_load_dwordx2 v[4:5], v3, s[16:19], 0 offen sc1\n"
"s_waitcnt vmcnt(0)\n"
"v_cmp_ne_u64 vcc, s[34:35], v[4:5]\n"
"s_nop 4\n"
"s_cbranch_vccnz .L_slow_go3%=\n"
".L_back_go3%=:\n"
"s_mov_b32 s28, 0\n"
"v_mov_b32 v4, s34\n"
"v_mov_b32 v5, s35\n"
"v_mov_b32 v6, s28\n"
"s_mov_b64 exec, 1\n"
"buffer_store_dwordx2 v[4:5], v6, s[16:19], 0 offen sc1\n"
"s_mov_b64 exec, -1\n"
".L_nouf%=:\n"
"s_waitcnt vmcnt(0)\n"
"ds_read_b64_tr_b16 a[96:97], v12 offset:16640\n"
"ds_read_b64_tr_b16 a[98:99], v12 offset:18688\n"
"ds_read_b64_tr_b16 a[100:101], v13 offset:16640\n"
"ds_read_b64_tr_b16 a[102:103], v13 offset:18688\n"
"ds_read_b64_tr_b16 a[104:105], v14 offset:16640\n"
"ds_read_b64_tr_b16 a[106:107], v14 offset:18688\n"
"ds_read_b64_tr_b16 a[108:109], v15 offset:16640\n"
"ds_read_b64_tr_b16 a[110:111], v15 offset:18688\n"
"ds_read_b64_tr_b16 a[112:113], v8 offset:16640\n"
"ds_read_b64_tr_b16 a[114:115], v8 offset:18688\n"
"ds_read_b64_tr_b16 a[116:117], v9 offset:16640\n"
"ds_read_b64_tr_b16 a[118:119], v9 offset:18688\n"
"ds_read_b64_tr_b16 a[120:121], v10 offset:16640\n"
"ds_read_b64_tr_b16 a[122:123], v10 offset:18688\n"
"ds_read_b64_tr_b16 a[124:125], v11 offset:16640\n"
"ds_read_b64_tr_b16 a[126:127], v11 offset:18688\n"
"s_waitcnt lgkmcnt(0)\n"
"s_add_u32 m0, s26, 0x14100\n"
"s_add_u32 s24, s86, 0x8000\n"
"s_cmp_ge_u32 s24, 0x70000\n"
"s_cselect_b32 s31, 0x70000, 0\n"
"s_sub_u32 s24, s24, s31\n"
"buffer_load_dwordx4 v30, s[40:43], s24 offen offset:0 lds\n"
"buffer_load_dwordx4 v31, s[40:43], s24 offen offset:1024 lds\n"
"buffer_load_dwordx4 v32, s[40:43], s24 offen offset:2048 lds\n"
"buffer_load_dwordx2 v[62:63], v126, s[16:19], 0 offen sc1\n"
"s_waitcnt vmcnt(0)\n"
"v_cmp_ne_u64 vcc, s[34:35], v[62:63]\n"
"s_nop 4\n"
"s_cbranch_vccnz .L_slow_u2%=\n"
".L_back_u2%=:\n"
"s_lshl_b32 s28, s6, 8\n"
"s_and_b32 s29, s7, 2\n"
"s_lshl_b32 s29, s29, 6\n"
"s_add_u32 s28, s28, s29\n"
"s_and_b32 s29, s7, 1\n"
"s_mul_i32 s29, s29, 1052672\n"
"s_add_u32 s28, s28, s29\n"
"s_add_u32 s28, s28, 0x100000\n"
"s_add_u32 s29, s28, 526336\n"
"v_lshrrev_b32 v28, 3, v0\n"
"v_mul_u32_u24 v28, 65792, v28\n"
"v_and_b32 v62, 7, v0\n"
"v_lshl_add_u32 v28, v62, 4, v28\n"
"buffer_load_dwordx4 a[212:215], v28, s[16:19], s28 offen sc1\n"
"buffer_load_dwordx4 a[216:219], v28, s[16:19], s29 offen sc1\n"
"s_lshl_b32 s28, s48, 6\n"
"s_lshl_b32 s29, s5, 4\n"
"s_add_u32 s28, s28, s29\n"
"v_add_u32 v3, s28, v2\n"
"v_lshlrev_b32 v3, 3, v3\n"
"v_lshrrev_b32 v36, 2, v0\n"
"v_add_u32 v50, s28, v36\n"
"v_lshlrev_b32 v50, 10, v50\n"
"v_and_b32 v37, 3, v0\n"
"v_lshl_add_u32 v50, v37, 4, v50\n"
"s_lshl_b32 s59, s49, 7\n"
"s_mov_b32 s28, 0x10000\n"
"buffer_load_dwordx2 v[48:49], v3, s[16:19], s28 offen sc1\n"
"buffer_load_dwordx4 v[40:43], v50, s[8:11], s59 offen offset:0\n"
"buffer_load_dwordx4 v[44:47], v50, s[8:11], s59 offen offset:64\n"
"v_mul_u32_u24 v36, 144, v36\n"
"v_lshl_add_u32 v36, v37, 4, v36\n"
"s_mul_i32 s28, s7, 2304\n"
"s_add_u32 s28, s28, 94464\n"
"v_add_u32 v36, s28, v36\n"
"v_mul_u32_u24 v37, 144, v2\n"
"v_lshl_add_u32 v37, v1, 3, v37\n"
"v_add_u32 v37, s28, v37\n"
"s_and_b32 s28, s7, 1\n"
"s_lshl_b32 s28, s28, 11\n"
"s_add_u32 s28, s28, s87\n"
"v_add_u32 v28, s28, v52\n"
"s_waitcnt vmcnt(3)\n"
"ds_write_b128 v28, a[212:215] offset:0\n"
"ds_write_b128 v28, a[216:219] offset:1024\n"
"s_waitcnt vmcnt(0)\n"
".L_common%=:\n"
"s_waitcnt lgkmcnt(0)\n"
"s_barrier\n"
"ds_read_b128 v[192:195], v35 offset:0\n"
"ds_read_b128 v[196:199], v35 offset:512\n"
"ds_read_b128 v[200:203], v35 offset:1024\n"
"ds_read_b128 v[204:207], v35 offset:1536\n"
"ds_read_b128 v[208:211], v35 offset:2048\n"
"ds_read_b128 v[212:215], v35 offset:2560\n"
"ds_read_b128 v[216:219], v35 offset:3072\n"
"ds_read_b128 v[220:223], v35 offset:3584\n"
"ds_read_b64 v[128:129], v34 offset:0\n"
"ds_read_b64 v[130:131], v34 offset:8\n"
"ds_read_b64 v[132:133], v34 offset:512\n"
"ds_read_b64 v[134:135], v34 offset:520\n"
"ds_read_b64 v[136:137], v34 offset:1024\n"
"ds_read_b64 v[138:139], v34 offset:1032\n"
"ds_read_b64 v[140:141], v34 offset:1536\n"
"ds_read_b64 v[142:143], v34 offset:1544\n"
"ds_read_b64 v[144:145], v34 offset:2048\n"
"ds_read_b64 v[146:147], v34 offset:2056\n"
"ds_read_b64 v[148:149], v34 offset:2560\n"
"ds_read_b64 v[150:151], v34 offset:2568\n"
"ds_read_b64 v[152:153], v34 offset:3072\n"
"ds_read_b64 v[154:155], v34 offset:3080\n"
"ds_read_b64 v[156:157], v34 offset:3584\n"
"ds_read_b64 v[158:159], v34 offset:3592\n"
"s_waitcnt lgkmcnt(15)\n"
"v_mfma_f32_16x16x32_bf16 a[252:255], v[24:27], v[192:195], 0\n"
"s_waitcnt lgkmcnt(15)\n"
"v_mfma_f32_16x16x32_bf16 a[248:251], v[24:27], v[196:199], 0\n"
"buffer_load_dwordx4 v33, s[40:43], s24 offen offset:3072 lds\n"
"s_add_u32 m0, s26, 0x4100\n"
"s_waitcnt lgkmcnt(15)\n"
"v_mfma_f32_16x16x32_bf16 a[244:247], v[24:27], v[200:203], 0\n"
"s_add_u32 s24, s86, 0x18000\n"
"s_cmp_ge_u32 s24, 0x70000\n"
"s_cselect_b32 s31, 0x70000, 0\n"
"s_sub_u32 s24, s24, s31\n"
"s_waitcnt lgkmcnt(15)\n"
"v_mfma_f32_16x16x32_bf16 a[240:243], v[24:27], v[204:207], 0\n"
"s_waitcnt lgkmcnt(15)\n"
"v_mfma_f32_16x16x32_bf16 a[236:239], v[24:27], v[208:211], 0\n"
"s_waitcnt lgkmcnt(15)\n"
"v_mfma_f32_16x16x32_bf16 a[232:235], v[24:27], v[212:215], 0\n"
"buffer_load_dwordx4 v30, s[40:43], s24 offen offset:0 lds\n"
"s_waitcnt vmcnt(19)\n"
"s_waitcnt lgkmcnt(15)\n"
"v_mfma_f32_16x16x32_bf16 a[228:231], v[24:27], v[216:219], 0\n"
"ds_read_b64_tr_b16 a[128:129], v8 offset:33024\n"
"ds_read_b64_tr_b16 a[130:131], v8 offset:35072\n"
"s_waitcnt lgkmcnt(15)\n"
"v_mfma_f32_16x16x32_bf16 a[224:227], v[24:27], v[220:223], 0\n"
"ds_read_b64_tr_b16 a[132:133], v9 offset:33024\n"
"ds_read_b64_tr_b16 a[134:135], v9 offset:35072\n"
"s_waitcnt lgkmcnt(15)\n"
"v_mfma_f32_16x16x32_bf16 a[252:255], v[128:131], v[192:195], a[252:255]\n"
"ds_read_b64_tr_b16 a[136:137], v10 offset:33024\n"
"ds_read_b64_tr_b16 a[138:139], v10 offset:35072\n"
"v_mfma_f32_16x16x32_bf16 a[248:251], v[128:131], v[196:199], a[248:251]\n"
"ds_read_b64_tr_b16 a[140:141], v11 offset:33024\n"
"ds_read_b64_tr_b16 a[142:143], v11 offset:35072\n"
"buffer_load_dwordx4 v31, s[40:43], s24 offen offset:1024 lds\n"
"v_mfma_f32_16x16x32_bf16 a[244:247], v[128:131], v[200:203], a[244:247]\n"
"v_mfma_f32_16x16x32_bf16 a[240:243], v[128:131], v[204:207], a[240:243]\n"
"v_mfma_f32_16x16x32_bf16 a[236:239], v[128:131], v[208:211], a[236:239]\n"
"v_mfma_f32_16x16x32_bf16 a[232:235], v[128:131], v[212:215], a[232:235]\n"
"buffer_load_dwordx4 v32, s[40:43], s24 offen offset:2048 lds\n"
"v_mfma_f32_16x16x32_bf16 a[228:231], v[128:131], v[216:219], a[228:231]\n"
"v_mfma_f32_16x16x32_bf16 a[224:227], v[128:131], v[220:223], a[224:227]\n"
"ds_read_b128 v[192:195], v35 offset:64\n"
"s_waitcnt lgkmcnt(15)\n"
"v_mfma_f32_16x16x32_bf16 a[252:255], v[132:135], v[196:199], a[252:255]\n"
"ds_read_b64 v[128:129], v34 offset:64\n"
"v_mfma_f32_16x16x32_bf16 a[248:251], v[132:135], v[200:203], a[248:251]\n"
"ds_read_b64 v[130:131], v34 offset:72\n"
"buffer_load_dwordx4 v33, s[40:43], s24 offen offset:3072 lds\n"
"v_mfma_f32_16x16x32_bf16 a[244:247], v[132:135], v[204:207], a[244:247]\n"
"s_waitcnt lgkmcnt(3)\n"
"s_add_u32 m0, s26, 0x8100\n"
"v_mfma_f32_16x16x32_bf16 a[240:243], v[132:135], v[208:211], a[240:243]\n"
"s_add_u32 s24, s86, 0x28000\n"
"s_cmp_ge_u32 s24, 0x70000\n"
"s_cselect_b32 s31, 0x70000, 0\n"
"s_sub_u32 s24, s24, s31\n"
"v_mfma_f32_16x16x32_bf16 a[236:239], v[132:135], v[212:215], a[236:239]\n"
"v_mfma_f32_16x16x32_bf16 a[232:235], v[132:135], v[216:219], a[232:235]\n"
"buffer_load_dwordx4 v30, s[40:43], s24 offen offset:0 lds\n"
"ds_read_b64_tr_b16 a[144:145], v12 offset:33024\n"
"v_mfma_f32_16x16x32_bf16 a[228:231], v[132:135], v[220:223], a[228:231]\n"
"ds_read_b128 v[196:199], v35 offset:576\n"
"ds_read_b64_tr_b16 a[146:147], v12 offset:35072\n"
"s_waitcnt lgkmcnt(15)\n"
"v_mfma_f32_16x16x32_bf16 a[252:255], v[136:139], v[200:203], a[252:255]\n"
"ds_read_b64 v[132:133], v34 offset:576\n"
"ds_read_b64_tr_b16 a[148:149], v13 offset:33024\n"
"ds_read_b64_tr_b16 a[150:151], v13 offset:35072\n"
"v_mfma_f32_16x16x32_bf16 a[248:251], v[136:139], v[204:207], a[248:251]\n"
"ds_read_b64 v[134:135], v34 offset:584\n"
"ds_read_b64_tr_b16 a[152:153], v14 offset:33024\n"
"v_mfma_f32_16x16x32_bf16 a[244:247], v[136:139], v[208:211], a[244:247]\n"
"ds_read_b64_tr_b16 a[154:155], v14 offset:35072\n"
"ds_read_b64_tr_b16 a[156:157], v15 offset:33024\n"
"v_mfma_f32_16x16x32_bf16 a[240:243], v[136:139], v[212:215], a[240:243]\n"
"ds_read_b64_tr_b16 a[158:159], v15 offset:35072\n"
"buffer_load_dwordx4 v31, s[40:43], s24 offen offset:1024 lds\n"
"v_mfma_f32_16x16x32_bf16 a[236:239], v[136:139], v[216:219], a[236:239]\n"
"v_mfma_f32_16x16x32_bf16 a[232:235], v[136:139], v[220:223], a[232:235]\n"
"ds_read_b128 v[200:203], v35 offset:1088\n"
"s_waitcnt lgkmcnt(15)\n"
"v_mfma_f32_16x16x32_bf16 a[252:255], v[140:143], v[204:207], a[252:255]\n"
"ds_read_b64 v[136:137], v34 offset:1088\n"
"s_waitcnt vmcnt(7)\n"
"v_mov_b32 v120, s34\n"
"v_mov_b32 v121, s35\n"
"v_mov_b32 v122, s21\n"
"s_mov_b64 exec, 1\n"
"buffer_store_dwordx2 v[120:121], v122, s[88:91], 0 offen sc1\n"
"s_mov_b64 exec, -1\n"
"v_mfma_f32_16x16x32_bf16 a[248:251], v[140:143], v[208:211], a[248:251]\n"
"ds_read_b64 v[138:139], v34 offset:1096\n"
"v_min_u32 v51, 6, v0\n"
"v_mfma_f32_16x16x32_bf16 a[244:247], v[140:143], v[212:215], a[244:247]\n"
"s_mul_i32 s28, s7, 7\n"
"v_add_u32 v51, s28, v51\n"
"v_mfma_f32_16x16x32_bf16 a[240:243], v[140:143], v[216:219], a[240:243]\n"
"v_lshrrev_b32 v53, 1, v51\n"
"v_and_b32 v51, 1, v51\n"
"v_mfma_f32_16x16x32_bf16 a[236:239], v[140:143], v[220:223], a[236:239]\n"
"ds_read_b128 v[64:67], v35 offset:3648\n"
"buffer_load_dwordx4 v32, s[40:43], s24 offen offset:2048 lds\n"
"s_waitcnt lgkmcnt(15)\n"
"v_mfma_f32_16x16x32_bf16 a[252:255], v[144:147], v[208:211], a[252:255]\n"
"ds_read_b128 v[204:207], v35 offset:1600\n"
"v_lshlrev_b32 v51, 3, v51\n"
"v_mfma_f32_16x16x32_bf16 a[248:251], v[144:147], v[212:215], a[248:251]\n"
"ds_read_b64 v[140:141], v34 offset:1600\n"
"v_lshl_add_u32 v51, v53, 6, v51\n"
"v_mfma_f32_16x16x32_bf16 a[244:247], v[144:147], v[216:219], a[244:247]\n"
"ds_read_b64 v[142:143], v34 offset:1608\n"
"s_lshr_b32 s28, s48, 1\n"
"v_mfma_f32_16x16x32_bf16 a[240:243], v[144:147], v[220:223], a[240:243]\n"
"ds_read_b128 v[208:211], v35 offset:2112\n"
"buffer_load_dwordx4 v33, s[40:43], s24 offen offset:3072 lds\n"
"s_waitcnt lgkmcnt(15)\n"
"v_mfma_f32_16x16x32_bf16 a[252:255], v[148:151], v[212:215], a[252:255]\n"
"ds_read_b64 v[144:145], v34 offset:2112\n"
"s_waitcnt lgkmcnt(9)\n"
"v_mfma_f32_16x16x32_bf16 a[248:251], v[148:151], v[216:219], a[248:251]\n"
"ds_read_b64 v[146:147], v34 offset:2120\n"
"s_add_u32 m0, s26, 0x18100\n"
"v_mfma_f32_16x16x32_bf16 a[244:247], v[148:151], v[220:223], a[244:247]\n"
"ds_read_b128 v[212:215], v35 offset:2624\n"
"s_add_u32 s24, s86, 0x38000\n"
"s_cmp_ge_u32 s24, 0x70000\n"
"s_cselect_b32 s31, 0x70000, 0\n"
"s_sub_u32 s24, s24, s31\n"
"s_waitcnt lgkmcnt(15)\n"
"v_mfma_f32_16x16x32_bf16 a[252:255], v[152:155], v[216:219], a[252:255]\n"
"ds_read_b64 v[148:149], v34 offset:2624\n"
"buffer_load_dwordx4 v30, s[40:43], s24 offen offset:0 lds\n"
"v_mfma_f32_16x16x32_bf16 a[248:251], v[152:155], v[220:223], a[248:251]\n"
"ds_read_b64 v[150:151], v34 offset:2632\n"
"ds_read_b128 v[216:219], v35 offset:3136\n"
"ds_read_b64_tr_b16 a[160:161], v8 offset:49408\n"
"ds_read_b64_tr_b16 a[162:163], v8 offset:51456\n"
"s_waitcnt lgkmcnt(15)\n"
"v_mfma_f32_16x16x32_bf16 a[252:255], v[156:159], v[220:223], a[252:255]\n"
"ds_read_b64 v[152:153], v34 offset:3136\n"
"ds_read_b64 v[156:157], v34 offset:3648\n"
"ds_read_b64_tr_b16 a[164:165], v9 offset:49408\n"
"s_waitcnt lgkmcnt(15)\n"
"v_mfma_f32_16x16x32_bf16 a[252:255], v[128:131], v[192:195], a[252:255]\n"
"ds_read_b64 v[154:155], v34 offset:3144\n"
"ds_read_b64 v[158:159], v34 offset:3656\n"
"ds_read_b64_tr_b16 a[166:167], v9 offset:51456\n"
"ds_read_b64_tr_b16 a[168:169], v10 offset:49408\n"
"s_waitcnt lgkmcnt(15)\n"
"v_mfma_f32_16x16x32_bf16 a[248:251], v[128:131], v[196:199], a[248:251]\n"
"ds_read_b64_tr_b16 a[170:171], v10 offset:51456\n"
"ds_read_b64_tr_b16 a[172:173], v11 offset:49408\n"
"v_mfma_f32_16x16x32_bf16 a[244:247], v[128:131], v[200:203], a[244:247]\n"
"ds_read_b64_tr_b16 a[174:175], v11 offset:51456\n"
"buffer_load_dwordx4 v31, s[40:43], s24 offen offset:1024 lds\n"
"v_mfma_f32_16x16x32_bf16 a[240:243], v[128:131], v[204:207], a[240:243]\n"
"s_mul_i32 s28, s28, 896\n"
"s_and_b32 s29, s48, 1\n"
"s_waitcnt lgkmcnt(15)\n"
"v_mfma_f32_16x16x32_bf16 a[236:239], v[128:131], v[208:211], a[236:239]\n"
"s_lshl_b32 s29, s29, 4\n"
"s_add_u32 s28, s28, s29\n"
"v_mfma_f32_16x16x32_bf16 a[232:235], v[128:131], v[212:215], a[232:235]\n"
"buffer_load_dwordx4 v32, s[40:43], s24 offen offset:2048 lds\n"
"s_add_u32 s28, s28, 0x4000\n"
"s_waitcnt lgkmcnt(12)\n"
"v_mfma_f32_16x16x32_bf16 a[228:231], v[128:131], v[216:219], a[228:231]\n"
"v_add_u32 v51, s28, v51\n"
"s_mul_i32 s28, s48, 224\n"
"v_mfma_f32_16x16x32_bf16 a[224:227], v[128:131], v[64:67], a[224:227]\n"
"ds_read_b128 v[192:195], v35 offset:128\n"
"s_mul_i32 s29, s7, 14\n"
"s_waitcnt lgkmcnt(15)\n"
"v_mfma_f32_16x16x32_bf16 a[252:255], v[132:135], v[196:199], a[252:255]\n"
"ds_read_b64 v[128:129], v34 offset:128\n"
"s_add_u32 s28, s28, s29\n"
"v_mfma_f32_16x16x32_bf16 a[248:251], v[132:135], v[200:203], a[248:251]\n"
"ds_read_b64 v[130:131], v34 offset:136\n"
"s_lshl_b32 s28, s28, 10\n"
"buffer_load_dwordx4 v33, s[40:43], s24 offen offset:3072 lds\n"
"v_mfma_f32_16x16x32_bf16 a[244:247], v[132:135], v[204:207], a[244:247]\n"
"s_add_u32 s28, s28, 0x580000\n"
"s_waitcnt lgkmcnt(3)\n"
"v_mfma_f32_16x16x32_bf16 a[240:243], v[132:135], v[208:211], a[240:243]\n"
"s_add_u32 s29, s5, 1\n"
"s_and_b32 s29, s29, 3\n"
"v_mfma_f32_16x16x32_bf16 a[236:239], v[132:135], v[212:215], a[236:239]\n"
"s_add_u32 m0, s26, 0xc100\n"
"s_mul_i32 s29, s29, 57344\n"
"v_mfma_f32_16x16x32_bf16 a[232:235], v[132:135], v[216:219], a[232:235]\n"
"s_add_u32 s24, s86, 0x48000\n"
"s_cmp_ge_u32 s24, 0x70000\n"
"s_cselect_b32 s31, 0x70000, 0\n"
"s_sub_u32 s24, s24, s31\n"
"buffer_load_dwordx4 v30, s[40:43], s24 offen offset:0 lds\n"
"s_add_u32 s22, s28, s29\n"
"v_mfma_f32_16x16x32_bf16 a[228:231], v[132:135], v[64:67], a[228:231]\n"
"ds_read_b128 v[196:199], v35 offset:640\n"
"ds_read_b64_tr_b16 a[176:177], v12 offset:256\n"
"s_add_u32 s29, s5, 2\n"
"ds_read_b64_tr_b16 a[178:179], v12 offset:2304\n"
"s_and_b32 s29, s29, 3\n"
"s_waitcnt lgkmcnt(15)\n"
"v_mfma_f32_16x16x32_bf16 a[252:255], v[136:139], v[200:203], a[252:255]\n"
"ds_read_b64 v[132:133], v34 offset:640\n"
"ds_read_b64_tr_b16 a[180:181], v13 offset:256\n"
"s_mul_i32 s29, s29, 57344\n"
"s_add_u32 s23, s28, s29\n"
"v_mfma_f32_16x16x32_bf16 a[248:251], v[136:139], v[204:207], a[248:251]\n"
"ds_read_b64 v[134:135], v34 offset:648\n"
"ds_read_b64_tr_b16 a[182:183], v13 offset:2304\n"
"s_add_u32 s29, s5, 3\n"
"ds_read_b64_tr_b16 a[184:185], v14 offset:256\n"
"s_and_b32 s29, s29, 3\n"
"v_mfma_f32_16x16x32_bf16 a[244:247], v[136:139], v[208:211], a[244:247]\n"
"ds_read_b64_tr_b16 a[186:187], v14 offset:2304\n"
"s_mul_i32 s29, s29, 57344\n"
"s_add_u32 s25, s28, s29\n"
"v_mfma_f32_16x16x32_bf16 a[240:243], v[136:139], v[212:215], a[240:243]\n"
"ds_read_b64_tr_b16 a[188:189], v15 offset:256\n"
"s_add_u32 s29, s5, 4\n"
"ds_read_b64_tr_b16 a[190:191], v15 offset:2304\n"
"s_and_b32 s29, s29, 3\n"
"buffer_load_dwordx4 v31, s[40:43], s24 offen offset:1024 lds\n"
"v_mfma_f32_16x16x32_bf16 a[236:239], v[136:139], v[216:219], a[236:239]\n"
"s_mul_i32 s29, s29, 57344\n"
"s_add_u32 s27, s28, s29\n"
"v_mfma_f32_16x16x32_bf16 a[232:235], v[136:139], v[64:67], a[232:235]\n"
"ds_read_b128 v[200:203], v35 offset:1152\n"
"s_lshr_b32 s29, s49, 1\n"
"s_mul_i32 s29, s29, 57344\n"
"s_waitcnt lgkmcnt(15)\n"
"v_mfma_f32_16x16x32_bf16 a[252:255], v[140:143], v[204:207], a[252:255]\n"
"ds_read_b64 v[136:137], v34 offset:1152\n"
"s_add_u32 s28, s28, s29\n"
"s_and_b32 s29, s49, 1\n"
"buffer_load_dwordx4 v32, s[40:43], s24 offen offset:2048 lds\n"
"v_mfma_f32_16x16x32_bf16 a[248:251], v[140:143], v[208:211], a[248:251]\n"
"ds_read_b64 v[138:139], v34 offset:1160\n"
"s_mul_i32 s29, s29, 7168\n"
"s_add_u32 s28, s28, s29\n"
"v_mfma_f32_16x16x32_bf16 a[244:247], v[140:143], v[212:215], a[244:247]\n"
"v_min_u32 v127, 55, v0\n"
"v_lshl_add_u32 v127, v127, 7, s28\n"
"v_mfma_f32_16x16x32_bf16 a[240:243], v[140:143], v[216:219], a[240:243]\n"
"s_lshr_b32 s28, s86, 5\n"
"s_add_u32 s29, s28, 0\n"
"s_cmp_ge_u32 s29, 14336\n"
"s_cselect_b32 s4, 14336, 0\n"
"s_sub_u32 s29, s29, s4\n"
"v_mfma_f32_16x16x32_bf16 a[236:239], v[140:143], v[64:67], a[236:239]\n"
"ds_read_b128 v[220:223], v35 offset:3712\n"
"v_add_u32 v120, s29, v52\n"
"buffer_load_dwordx4 v33, s[40:43], s24 offen offset:3072 lds\n"
"s_waitcnt lgkmcnt(15)\n"
"v_mfma_f32_16x16x32_bf16 a[252:255], v[144:147], v[208:211], a[252:255]\n"
"ds_read_b128 v[204:207], v35 offset:1664\n"
"s_add_u32 s29, s28, 2048\n"
"s_cmp_ge_u32 s29, 14336\n"
"s_cselect_b32 s4, 14336, 0\n"
"s_sub_u32 s29, s29, s4\n"
"v_mfma_f32_16x16x32_bf16 a[248:251], v[144:147], v[212:215], a[248:251]\n"
"ds_read_b64 v[140:141], v34 offset:1664\n"
"s_waitcnt lgkmcnt(6)\n"
"v_mfma_f32_16x16x32_bf16 a[244:247], v[144:147], v[216:219], a[244:247]\n"
"ds_read_b64 v[142:143], v34 offset:1672\n"
"v_add_u32 v121, s29, v52\n"
"v_mfma_f32_16x16x32_bf16 a[240:243], v[144:147], v[64:67], a[240:243]\n"
"ds_read_b128 v[208:211], v35 offset:2176\n"
"s_add_u32 m0, s26, 0x10100\n"
"s_add_u32 s29, s28, 4096\n"
"s_cmp_ge_u32 s29, 14336\n"
"s_cselect_b32 s4, 14336, 0\n"
"s_sub_u32 s29, s29, s4\n"
"s_add_u32 s24, s86, 0x58000\n"
"s_cmp_ge_u32 s24, 0x70000\n"
"s_cselect_b32 s31, 0x70000, 0\n"
"s_sub_u32 s24, s24, s31\n"
"buffer_load_dwordx4 v30, s[40:43], s24 offen offset:0 lds\n"
"ds_read_b64_tr_b16 a[192:193], v12 offset:49408\n"
"s_waitcnt lgkmcnt(15)\n"
"v_mfma_f32_16x16x32_bf16 a[252:255], v[148:151], v[212:215], a[252:255]\n"
"ds_read_b64 v[144:145], v34 offset:2176\n"
"ds_read_b64_tr_b16 a[194:195], v12 offset:51456\n"
"v_add_u32 v122, s29, v52\n"
"v_mfma_f32_16x16x32_bf16 a[248:251], v[148:151], v[216:219], a[248:251]\n"
"ds_read_b64 v[146:147], v34 offset:2184\n"
"ds_read_b64_tr_b16 a[196:197], v13 offset:49408\n"
"s_add_u32 s29, s28, 6144\n"
"s_cmp_ge_u32 s29, 14336\n"
"s_cselect_b32 s4, 14336, 0\n"
"s_sub_u32 s29, s29, s4\n"
"ds_read_b64_tr_b16 a[198:199], v13 offset:51456\n"
"v_mfma_f32_16x16x32_bf16 a[244:247], v[148:151], v[64:67], a[244:247]\n"
"ds_read_b128 v[212:215], v35 offset:2688\n"
"ds_read_b64_tr_b16 a[200:201], v14 offset:49408\n"
"v_add_u32 v123, s29, v52\n"
"s_waitcnt lgkmcnt(15)\n"
"v_mfma_f32_16x16x32_bf16 a[252:255], v[152:155], v[216:219], a[252:255]\n"
"ds_read_b64 v[148:149], v34 offset:2688\n"
"ds_read_b64_tr_b16 a[202:203], v14 offset:51456\n"
"s_add_u32 s29, s28, 8192\n"
"s_cmp_ge_u32 s29, 14336\n"
"s_cselect_b32 s4, 14336, 0\n"
"s_sub_u32 s29, s29, s4\n"
"ds_read_b64_tr_b16 a[204:205], v15 offset:49408\n"
"v_mfma_f32_16x16x32_bf16 a[248:251], v[152:155], v[64:67], a[248:251]\n"
"ds_read_b64 v[150:151], v34 offset:2696\n"
"ds_read_b128 v[216:219], v35 offset:3200\n"
"v_add_u32 v124, s29, v52\n"
"ds_read_b64_tr_b16 a[206:207], v15 offset:51456\n"
"buffer_load_dwordx4 v31, s[40:43], s24 offen offset:1024 lds\n"
"s_waitcnt lgkmcnt(15)\n"
"v_mfma_f32_16x16x32_bf16 a[252:255], v[156:159], v[64:67], a[252:255]\n"
"ds_read_b64 v[152:153], v34 offset:3200\n"
"ds_read_b64 v[156:157], v34 offset:3712\n"
"s_add_u32 s29, s28, 10240\n"
"s_cmp_ge_u32 s29, 14336\n"
"s_cselect_b32 s4, 14336, 0\n"
"s_sub_u32 s29, s29, s4\n"
"s_waitcnt lgkmcnt(15)\n"
"v_mfma_f32_16x16x32_bf16 a[252:255], v[128:131], v[192:195], a[252:255]\n"
"ds_read_b64 v[154:155], v34 offset:3208\n"
"ds_read_b64 v[158:159], v34 offset:3720\n"
"v_add_u32 v125, s29, v52\n"
"s_waitcnt lgkmcnt(15)\n"
"v_mfma_f32_16x16x32_bf16 a[248:251], v[128:131], v[196:199], a[248:251]\n"
"s_add_u32 s29, s28, 12288\n"
"s_cmp_ge_u32 s29, 14336\n"
"s_cselect_b32 s4, 14336, 0\n"
"s_sub_u32 s29, s29, s4\n"
"v_mfma_f32_16x16x32_bf16 a[244:247], v[128:131], v[200:203], a[244:247]\n"
"v_add_u32 v126, s29, v52\n"
"buffer_load_dwordx2 v[62:63], v51, s[16:19], 0 offen sc1\n"
"v_mfma_f32_16x16x32_bf16 a[240:243], v[128:131], v[204:207], a[240:243]\n"
"s_waitcnt lgkmcnt(11)\n"
"v_mfma_f32_16x16x32_bf16 a[236:239], v[128:131], v[208:211], a[236:239]\n"
"v_mfma_f32_16x16x32_bf16 a[232:235], v[128:131], v[212:215], a[232:235]\n"
"s_waitcnt lgkmcnt(5)\n"
"v_mfma_f32_16x16x32_bf16 a[228:231], v[128:131], v[216:219], a[228:231]\n"
"v_mfma_f32_16x16x32_bf16 a[224:227], v[128:131], v[220:223], a[224:227]\n"
"ds_read_b128 v[192:195], v35 offset:192\n"
"s_waitcnt lgkmcnt(15)\n"
"v_mfma_f32_16x16x32_bf16 a[252:255], v[132:135], v[196:199], a[252:255]\n"
"ds_read_b64 v[128:129], v34 offset:192\n"
"v_mfma_f32_16x16x32_bf16 a[248:251], v[132:135], v[200:203], a[248:251]\n"
"ds_read_b64 v[130:131], v34 offset:200\n"
"buffer_load_dwordx4 v32, s[40:43], s24 offen offset:2048 lds\n"
"v_mfma_f32_16x16x32_bf16 a[244:247], v[132:135], v[204:207], a[244:247]\n"
"v_mfma_f32_16x16x32_bf16 a[240:243], v[132:135], v[208:211], a[240:243]\n"
"v_mfma_f32_16x16x32_bf16 a[236:239], v[132:135], v[212:215], a[236:239]\n"
"v_mfma_f32_16x16x32_bf16 a[232:235], v[132:135], v[216:219], a[232:235]\n"
"buffer_load_dwordx4 v33, s[40:43], s24 offen offset:3072 lds\n"
"s_waitcnt lgkmcnt(7)\n"
"v_mfma_f32_16x16x32_bf16 a[228:231], v[132:135], v[220:223], a[228:231]\n"
"ds_read_b128 v[196:199], v35 offset:704\n"
"s_add_u32 m0, s26, 0x1c100\n"
"s_waitcnt lgkmcnt(15)\n"
"v_mfma_f32_16x16x32_bf16 a[252:255], v[136:139], v[200:203], a[252:255]\n"
"ds_read_b64 v[132:133], v34 offset:704\n"
"s_add_u32 s24, s86, 0x68000\n"
"s_cmp_ge_u32 s24, 0x70000\n"
"s_cselect_b32 s31, 0x70000, 0\n"
"s_sub_u32 s24, s24, s31\n"
"v_mfma_f32_16x16x32_bf16 a[248:251], v[136:139], v[204:207], a[248:251]\n"
"ds_read_b64 v[134:135], v34 offset:712\n"
"v_mfma_f32_16x16x32_bf16 a[244:247], v[136:139], v[208:211], a[244:247]\n"
"buffer_load_dwordx4 v30, s[40:43], s24 offen offset:0 lds\n"
"v_mfma_f32_16x16x32_bf16 a[240:243], v[136:139], v[212:215], a[240:243]\n"
"v_mfma_f32_16x16x32_bf16 a[236:239], v[136:139], v[216:219], a[236:239]\n"
"v_mfma_f32_16x16x32_bf16 a[232:235], v[136:139], v[220:223], a[232:235]\n"
"ds_read_b128 v[200:203], v35 offset:1216\n"
"s_waitcnt lgkmcnt(15)\n"
"v_mfma_f32_16x16x32_bf16 a[252:255], v[140:143], v[204:207], a[252:255]\n"
"ds_read_b64 v[136:137], v34 offset:1216\n"
"buffer_load_dwordx4 v31, s[40:43], s24 offen offset:1024 lds\n"
"v_mfma_f32_16x16x32_bf16 a[248:251], v[140:143], v[208:211], a[248:251]\n"
"ds_read_b64 v[138:139], v34 offset:1224\n"
"v_mfma_f32_16x16x32_bf16 a[244:247], v[140:143], v[212:215], a[244:247]\n"
"v_mfma_f32_16x16x32_bf16 a[240:243], v[140:143], v[216:219], a[240:243]\n"
"s_waitcnt vmcnt(4)\n"
"v_cmp_ne_u64 vcc, s[34:35], v[62:63]\n"
"s_nop 4\n"
"s_cbranch_vccnz .L_slow_h%=\n"
".L_back_h%=:\n"
"v_mfma_f32_16x16x32_bf16 a[236:239], v[140:143], v[220:223], a[236:239]\n"
"ds_read_b128 v[64:67], v35 offset:3776\n"
"buffer_load_dword v3, v127, s[16:19], 0 offen sc1\n"
"s_waitcnt lgkmcnt(15)\n"
"v_mfma_f32_16x16x32_bf16 a[252:255], v[144:147], v[208:211], a[252:255]\n"
"ds_read_b128 v[204:207], v35 offset:1728\n"
"v_mfma_f32_16x16x32_bf16 a[248:251], v[144:147], v[212:215], a[248:251]\n"
"ds_read_b64 v[140:141], v34 offset:1728\n"
"v_mfma_f32_16x16x32_bf16 a[244:247], v[144:147], v[216:219], a[244:247]\n"
"ds_read_b64 v[142:143], v34 offset:1736\n"
"v_mfma_f32_16x16x32_bf16 a[240:243], v[144:147], v[220:223], a[240:243]\n"
"ds_read_b128 v[208:211], v35 offset:2240\n"
"s_waitcnt lgkmcnt(15)\n"
"v_mfma_f32_16x16x32_bf16 a[252:255], v[148:151], v[212:215], a[252:255]\n"
"ds_read_b64 v[144:145], v34 offset:2240\n"
"v_mfma_f32_16x16x32_bf16 a[248:251], v[148:151], v[216:219], a[248:251]\n"
"ds_read_b64 v[146:147], v34 offset:2248\n"
"v_mfma_f32_16x16x32_bf16 a[244:247], v[148:151], v[220:223], a[244:247]\n"
"ds_read_b128 v[212:215], v35 offset:2752\n"
"s_waitcnt lgkmcnt(15)\n"
"v_mfma_f32_16x16x32_bf16 a[252:255], v[152:155], v[216:219], a[252:255]\n"
"ds_read_b64 v[148:149], v34 offset:2752\n"
"v_mfma_f32_16x16x32_bf16 a[248:251], v[152:155], v[220:223], a[248:251]\n"
"ds_read_b64 v[150:151], v34 offset:2760\n"
"ds_read_b128 v[216:219], v35 offset:3264\n"
"s_waitcnt lgkmcnt(15)\n"
"v_mfma_f32_16x16x32_bf16 a[252:255], v[156:159], v[220:223], a[252:255]\n"
"ds_read_b64 v[152:153], v34 offset:3264\n"
"ds_read_b64 v[156:157], v34 offset:3776\n"
"s_waitcnt lgkmcnt(15)\n"
"v_mfma_f32_16x16x32_bf16 a[252:255], v[128:131], v[192:195], a[252:255]\n"
"ds_read_b64 v[154:155], v34 offset:3272\n"
"ds_read_b64 v[158:159], v34 offset:3784\n"
"s_waitcnt lgkmcnt(13)\n"
"v_mfma_f32_16x16x32_bf16 a[248:251], v[128:131], v[196:199], a[248:251]\n"
"v_mfma_f32_16x16x32_bf16 a[244:247], v[128:131], v[200:203], a[244:247]\n"
"v_mfma_f32_16x16x32_bf16 a[240:243], v[128:131], v[204:207], a[240:243]\n"
"s_waitcnt lgkmcnt(7)\n"
"v_mfma_f32_16x16x32_bf16 a[236:239], v[128:131], v[208:211], a[236:239]\n"
"v_mfma_f32_16x16x32_bf16 a[232:235], v[128:131], v[212:215], a[232:235]\n"
"buffer_load_dwordx4 v[68:71], v120, s[16:19], s22 offen offset:0 sc1\n"
"s_waitcnt lgkmcnt(4)\n"
"v_mfma_f32_16x16x32_bf16 a[228:231], v[128:131], v[216:219], a[228:231]\n"
"v_mfma_f32_16x16x32_bf16 a[224:227], v[128:131], v[64:67], a[224:227]\n"
"s_waitcnt lgkmcnt(15)\n"
"v_mfma_f32_16x16x32_bf16 a[252:255], v[132:135], v[196:199], a[252:255]\n"
"v_mfma_f32_16x16x32_bf16 a[248:251], v[132:135], v[200:203], a[248:251]\n"
"buffer_load_dwordx4 v32, s[40:43], s24 offen offset:2048 lds\n"
"v_mfma_f32_16x16x32_bf16 a[244:247], v[132:135], v[204:207], a[244:247]\n"
"v_mfma_f32_16x16x32_bf16 a[240:243], v[132:135], v[208:211], a[240:243]\n"
"v_mfma_f32_16x16x32_bf16 a[236:239], v[132:135], v[212:215], a[236:239]\n"
"v_mfma_f32_16x16x32_bf16 a[232:235], v[132:135], v[216:219], a[232:235]\n"
"buffer_load_dwordx4 v[72:75], v121, s[16:19], s22 offen offset:0 sc1\n"
"v_mfma_f32_16x16x32_bf16 a[228:231], v[132:135], v[64:67], a[228:231]\n"
"s_waitcnt lgkmcnt(15)\n"
"v_mfma_f32_16x16x32_bf16 a[252:255], v[136:139], v[200:203], a[252:255]\n"
"v_mfma_f32_16x16x32_bf16 a[248:251], v[136:139], v[204:207], a[248:251]\n"
"v_mfma_f32_16x16x32_bf16 a[244:247], v[136:139], v[208:211], a[244:247]\n"
"buffer_load_dwordx4 v33, s[40:43], s24 offen offset:3072 lds\n"
"s_waitcnt vmcnt(20)\n"
"v_mfma_f32_16x16x32_bf16 a[240:243], v[136:139], v[212:215], a[240:243]\n"
"ds_read_b64_tr_b16 a[0:1], v12 offset:16640\n"
"ds_read_b64_tr_b16 a[2:3], v12 offset:18688\n"
"v_mfma_f32_16x16x32_bf16 a[236:239], v[136:139], v[216:219], a[236:239]\n"
"ds_read_b64_tr_b16 a[4:5], v13 offset:16640\n"
"ds_read_b64_tr_b16 a[6:7], v13 offset:18688\n"
"v_mfma_f32_16x16x32_bf16 a[232:235], v[136:139], v[64:67], a[232:235]\n"
"ds_read_b64_tr_b16 a[8:9], v14 offset:16640\n"
"ds_read_b64_tr_b16 a[10:11], v14 offset:18688\n"
"s_waitcnt lgkmcnt(15)\n"
"v_mfma_f32_16x16x32_bf16 a[252:255], v[140:143], v[204:207], a[252:255]\n"
"buffer_load_dwordx4 v[76:79], v122, s[16:19], s22 offen offset:0 sc1\n"
"ds_read_b64_tr_b16 a[12:13], v15 offset:16640\n"
"v_mfma_f32_16x16x32_bf16 a[248:251], v[140:143], v[208:211], a[248:251]\n"
"ds_read_b64_tr_b16 a[14:15], v15 offset:18688\n"
"ds_read_b64_tr_b16 a[16:17], v8 offset:16640\n"
"v_mfma_f32_16x16x32_bf16 a[244:247], v[140:143], v[212:215], a[244:247]\n"
"ds_read_b64_tr_b16 a[18:19], v8 offset:18688\n"
"ds_read_b64_tr_b16 a[20:21], v9 offset:16640\n"
"v_mfma_f32_16x16x32_bf16 a[240:243], v[140:143], v[216:219], a[240:243]\n"
"ds_read_b64_tr_b16 a[22:23], v9 offset:18688\n"
"ds_read_b64_tr_b16 a[24:25], v10 offset:16640\n"
"v_mfma_f32_16x16x32_bf16 a[236:239], v[140:143], v[64:67], a[236:239]\n"
"buffer_load_dwordx4 v[80:83], v123, s[16:19], s22 offen offset:0 sc1\n"
"ds_read_b64_tr_b16 a[26:27], v10 offset:18688\n"
"s_waitcnt lgkmcnt(15)\n"
"v_mfma_f32_16x16x32_bf16 a[252:255], v[144:147], v[208:211], a[252:255]\n"
"ds_read_b64_tr_b16 a[28:29], v11 offset:16640\n"
"ds_read_b64_tr_b16 a[30:31], v11 offset:18688\n"
"v_mfma_f32_16x16x32_bf16 a[248:251], v[144:147], v[212:215], a[248:251]\n"
"ds_read_b64_tr_b16 a[32:33], v8 offset:33024\n"
"ds_read_b64_tr_b16 a[34:35], v8 offset:35072\n"
"v_mfma_f32_16x16x32_bf16 a[244:247], v[144:147], v[216:219], a[244:247]\n"
"ds_read_b64_tr_b16 a[36:37], v9 offset:33024\n"
"ds_read_b64_tr_b16 a[38:39], v9 offset:35072\n"
"v_mfma_f32_16x16x32_bf16 a[240:243], v[144:147], v[64:67], a[240:243]\n"
"buffer_load_dwordx4 v[84:87], v124, s[16:19], s22 offen offset:0 sc1\n"
"ds_read_b64_tr_b16 a[40:41], v10 offset:33024\n"
"s_waitcnt lgkmcnt(15)\n"
"v_mfma_f32_16x16x32_bf16 a[252:255], v[148:151], v[212:215], a[252:255]\n"
"ds_read_b64_tr_b16 a[42:43], v10 offset:35072\n"
"ds_read_b64_tr_b16 a[44:45], v11 offset:33024\n"
"v_mfma_f32_16x16x32_bf16 a[248:251], v[148:151], v[216:219], a[248:251]\n"
"ds_read_b64_tr_b16 a[46:47], v11 offset:35072\n"
"v_mfma_f32_16x16x32_bf16 a[244:247], v[148:151], v[64:67], a[244:247]\n"
"s_waitcnt lgkmcnt(15)\n"
"v_mfma_f32_16x16x32_bf16 a[252:255], v[152:155], v[216:219], a[252:255]\n"
"buffer_load_dwordx4 v[88:91], v125, s[16:19], s22 offen offset:0 sc1\n"
"v_mfma_f32_16x16x32_bf16 a[248:251], v[152:155], v[64:67], a[248:251]\n"
"s_waitcnt lgkmcnt(15)\n"
"v_mfma_f32_16x16x32_bf16 a[252:255], v[156:159], v[64:67], a[252:255]\n"
"s_barrier\n"
"s_lshl_b32 s28, s7, 12\n"
"v_lshl_add_u32 v16, v0, 4, s28\n"
"v_xor_b32 v17, 0x1000, v16\n"
"s_and_b32 s28, s7, 1\n"
"s_cmp_eq_u32 s28, 0\n"
"s_cbranch_scc1 .L_hf0%=\n"
"ds_write_b128 v16, a[224:227] offset:0\n"
"ds_write_b128 v16, a[228:231] offset:1024\n"
"ds_write_b128 v16, a[232:235] offset:2048\n"
"ds_write_b128 v16, a[236:239] offset:3072\n"
"v_accvgpr_read_b32 v192, a240\n"
"v_accvgpr_read_b32 v193, a241\n"
"v_accvgpr_read_b32 v194, a242\n"
"v_accvgpr_read_b32 v195, a243\n"
"v_accvgpr_read_b32 v196, a244\n"
"v_accvgpr_read_b32 v197, a245\n"
"v_accvgpr_read_b32 v198, a246\n"
"v_accvgpr_read_b32 v199, a247\n"
"v_accvgpr_read_b32 v200, a248\n"
"v_accvgpr_read_b32 v201, a249\n"
"v_accvgpr_read_b32 v202, a250\n"
"v_accvgpr_read_b32 v203, a251\n"
"v_accvgpr_read_b32 v204, a252\n"
"v_accvgpr_read_b32 v205, a253\n"
"v_accvgpr_read_b32 v206, a254\n"
"v_accvgpr_read_b32 v207, a255\n"
"s_branch .L_hf1%=\n"
".L_hf0%=:\n"
"s_nop 7\n"
"ds_write_b128 v16, a[240:243] offset:0\n"
"ds_write_b128 v16, a[244:247] offset:1024\n"
"ds_write_b128 v16, a[248:251] offset:2048\n"
"ds_write_b128 v16, a[252:255] offset:3072\n"
"v_accvgpr_read_b32 v192, a224\n"
"v_accvgpr_read_b32 v193, a225\n"
"v_accvgpr_read_b32 v194, a226\n"
"v_accvgpr_read_b32 v195, a227\n"
"v_accvgpr_read_b32 v196, a228\n"
"v_accvgpr_read_b32 v197, a229\n"
"v_accvgpr_read_b32 v198, a230\n"
"v_accvgpr_read_b32 v199, a231\n"
"v_accvgpr_read_b32 v200, a232\n"
"v_accvgpr_read_b32 v201, a233\n"
"v_accvgpr_read_b32 v202, a234\n"
"v_accvgpr_read_b32 v203, a235\n"
"v_accvgpr_read_b32 v204, a236\n"
"v_accvgpr_read_b32 v205, a237\n"
"v_accvgpr_read_b32 v206, a238\n"
"v_accvgpr_read_b32 v207, a239\n"
".L_hf1%=:\n"
"buffer_load_dwordx4 v[92:95], v126, s[16:19], s22 offen offset:0 sc1\n"
"buffer_load_dwordx4 v[96:99], v120, s[16:19], s22 offen offset:1024 sc1\n"
"s_waitcnt lgkmcnt(0)\n"
"s_barrier\n"
"ds_read_b128 v[208:211], v17 offset:0\n"
"ds_read_b128 v[212:215], v17 offset:1024\n"
"ds_read_b128 v[216:219], v17 offset:2048\n"
"ds_read_b128 v[220:223], v17 offset:3072\n"
"s_lshl_b32 s28, s6, 1\n"
"s_lshr_b32 s29, s7, 1\n"
"s_add_u32 s28, s28, s29\n"
"s_mul_i32 s28, s28, 4224\n"
"s_and_b32 s29, s7, 1\n"
"s_lshl_b32 s29, s29, 11\n"
"s_add_u32 s28, s28, s29\n"
"s_add_u32 s28, s28, 0x340000\n"
"v_lshlrev_b32 v16, 5, v2\n"
"v_lshl_add_u32 v16, v1, 3, v16\n"
"buffer_load_dwordx4 v[100:103], v121, s[16:19], s22 offen offset:1024 sc1\n"
"buffer_load_dwordx4 v[104:107], v122, s[16:19], s22 offen offset:1024 sc1\n"
"s_waitcnt lgkmcnt(0)\n"
"v_pk_add_f32 v[192:193], v[192:193], v[208:209]\n"
"v_pk_add_f32 v[194:195], v[194:195], v[210:211]\n"
"v_pk_add_f32 v[196:197], v[196:197], v[212:213]\n"
"v_pk_add_f32 v[198:199], v[198:199], v[214:215]\n"
"v_pk_add_f32 v[200:201], v[200:201], v[216:217]\n"
"v_pk_add_f32 v[202:203], v[202:203], v[218:219]\n"
"v_pk_add_f32 v[204:205], v[204:205], v[220:221]\n"
"v_pk_add_f32 v[206:207], v[206:207], v[222:223]\n"
"v_cvt_pk_bf16_f32 v54, v192, v193\n"
"v_cvt_pk_bf16_f32 v55, v194, v195\n"
"v_cvt_pk_bf16_f32 v56, v196, v197\n"
"v_cvt_pk_bf16_f32 v57, v198, v199\n"
"v_cvt_pk_bf16_f32 v58, v200, v201\n"
"v_cvt_pk_bf16_f32 v59, v202, v203\n"
"v_cvt_pk_bf16_f32 v60, v204, v205\n"
"v_cvt_pk_bf16_f32 v61, v206, v207\n"
"buffer_store_dwordx2 v[54:55], v16, s[16:19], s28 offen offset:0 sc1\n"
"buffer_store_dwordx2 v[56:57], v16, s[16:19], s28 offen offset:512 sc1\n"
"buffer_store_dwordx2 v[58:59], v16, s[16:19], s28 offen offset:1024 sc1\n"
"buffer_store_dwordx2 v[60:61], v16, s[16:19], s28 offen offset:1536 sc1\n"
"buffer_load_dwordx4 v[108:111], v123, s[16:19], s22 offen offset:1024 sc1\n"
"buffer_load_dwordx4 v[112:115], v124, s[16:19], s22 offen offset:1024 sc1\n"
"buffer_load_dwordx4 v[116:119], v125, s[16:19], s22 offen offset:1024 sc1\n"
"buffer_load_dwordx4 v[160:163], v126, s[16:19], s22 offen offset:1024 sc1\n"
"s_waitcnt vmcnt(19)\n"
"v_mfma_f32_16x16x32_bf16 v[192:195], a[96:99], v[68:71], 0\n"
"s_lshl_b32 s28, s6, 6\n"
"s_lshl_b32 s29, s7, 3\n"
"v_mfma_f32_16x16x32_bf16 v[196:199], a[100:103], v[68:71], 0\n"
"buffer_load_dwordx4 v[164:167], v120, s[16:19], s23 offen offset:0 sc1\n"
"s_add_u32 s28, s28, s29\n"
"v_mfma_f32_16x16x32_bf16 v[200:203], a[104:107], v[68:71], 0\n"
"s_waitcnt vmcnt(29)\n"
"ds_read_b64_tr_b16 a[48:49], v12 offset:33024\n"
"v_mfma_f32_16x16x32_bf16 v[204:207], a[108:111], v[68:71], 0\n"
"ds_read_b64_tr_b16 a[50:51], v12 offset:35072\n"
"ds_read_b64_tr_b16 a[52:53], v13 offset:33024\n"
"s_waitcnt vmcnt(18)\n"
"v_mfma_f32_16x16x32_bf16 v[192:195], a[112:115], v[72:75], v[192:195]\n"
"ds_read_b64_tr_b16 a[54:55], v13 offset:35072\n"
"ds_read_b64_tr_b16 a[56:57], v14 offset:33024\n"
"v_mfma_f32_16x16x32_bf16 v[196:199], a[116:119], v[72:75], v[196:199]\n"
"buffer_load_dwordx4 v[168:171], v121, s[16:19], s23 offen offset:0 sc1\n"
"ds_read_b64_tr_b16 a[58:59], v14 offset:35072\n"
"v_mfma_f32_16x16x32_bf16 v[200:203], a[120:123], v[72:75], v[200:203]\n"
"ds_read_b64_tr_b16 a[60:61], v15 offset:33024\n"
"ds_read_b64_tr_b16 a[62:63], v15 offset:35072\n"
"v_mfma_f32_16x16x32_bf16 v[204:207], a[124:127], v[72:75], v[204:207]\n"
"ds_read_b64_tr_b16 a[64:65], v8 offset:49408\n"
"ds_read_b64_tr_b16 a[66:67], v8 offset:51456\n"
"s_waitcnt vmcnt(17)\n"
"v_mfma_f32_16x16x32_bf16 v[192:195], a[128:131], v[76:79], v[192:195]\n"
"ds_read_b64_tr_b16 a[68:69], v9 offset:49408\n"
"ds_read_b64_tr_b16 a[70:71], v9 offset:51456\n"
"v_mfma_f32_16x16x32_bf16 v[196:199], a[132:135], v[76:79], v[196:199]\n"
"buffer_load_dwordx4 v[172:175], v122, s[16:19], s23 offen offset:0 sc1\n"
"ds_read_b64_tr_b16 a[72:73], v10 offset:49408\n"
"v_mfma_f32_16x16x32_bf16 v[200:203], a[136:139], v[76:79], v[200:203]\n"
"ds_read_b64_tr_b16 a[74:75], v10 offset:51456\n"
"ds_read_b64_tr_b16 a[76:77], v11 offset:49408\n"
"v_mfma_f32_16x16x32_bf16 v[204:207], a[140:143], v[76:79], v[204:207]\n"
"ds_read_b64_tr_b16 a[78:79], v11 offset:51456\n"
"s_add_u32 s28, s28, 0x8000\n"
"s_waitcnt vmcnt(17)\n"
"v_mfma_f32_16x16x32_bf16 v[192:195], a[144:147], v[80:83], v[192:195]\n"
"s_waitcnt vmcnt(7)\n"
"v_mfma_f32_16x16x32_bf16 v[196:199], a[148:151], v[80:83], v[196:199]\n"
"buffer_load_dwordx4 v[176:179], v123, s[16:19], s23 offen offset:0 sc1\n"
"v_mfma_f32_16x16x32_bf16 v[200:203], a[152:155], v[80:83], v[200:203]\n"
"v_mfma_f32_16x16x32_bf16 v[204:207], a[156:159], v[80:83], v[204:207]\n"
"s_waitcnt vmcnt(17)\n"
"v_mfma_f32_16x16x32_bf16 v[192:195], a[160:163], v[84:87], v[192:195]\n"
"s_waitcnt vmcnt(20)\n"
"ds_read_b64_tr_b16 a[80:81], v12 offset:256\n"
"v_mfma_f32_16x16x32_bf16 v[196:199], a[164:167], v[84:87], v[196:199]\n"
"buffer_load_dwordx4 v[180:183], v124, s[16:19], s23 offen offset:0 sc1\n"
"ds_read_b64_tr_b16 a[82:83], v12 offset:2304\n"
"v_mfma_f32_16x16x32_bf16 v[200:203], a[168:171], v[84:87], v[200:203]\n"
"ds_read_b64_tr_b16 a[84:85], v13 offset:256\n"
"ds_read_b64_tr_b16 a[86:87], v13 offset:2304\n"
"v_mfma_f32_16x16x32_bf16 v[204:207], a[172:175], v[84:87], v[204:207]\n"
"ds_read_b64_tr_b16 a[88:89], v14 offset:256\n"
"ds_read_b64_tr_b16 a[90:91], v14 offset:2304\n"
"s_waitcnt vmcnt(17)\n"
"v_mfma_f32_16x16x32_bf16 v[192:195], a[176:179], v[88:91], v[192:195]\n"
"ds_read_b64_tr_b16 a[92:93], v15 offset:256\n"
"ds_read_b64_tr_b16 a[94:95], v15 offset:2304\n"
"v_mfma_f32_16x16x32_bf16 v[196:199], a[180:183], v[88:91], v[196:199]\n"
"buffer_load_dwordx4 v[184:187], v125, s[16:19], s23 offen offset:0 sc1\n"
"ds_read_b64_tr_b16 a[208:209], v12 offset:49408\n"
"v_mfma_f32_16x16x32_bf16 v[200:203], a[184:187], v[88:91], v[200:203]\n"
"ds_read_b64_tr_b16 a[210:211], v12 offset:51456\n"
"ds_read_b64_tr_b16 a[212:213], v13 offset:49408\n"
"v_mfma_f32_16x16x32_bf16 v[204:207], a[188:191], v[88:91], v[204:207]\n"
"ds_read_b64_tr_b16 a[214:215], v13 offset:51456\n"
"ds_read_b64_tr_b16 a[216:217], v14 offset:49408\n"
"s_waitcnt vmcnt(17)\n"
"v_mfma_f32_16x16x32_bf16 v[192:195], a[192:195], v[92:95], v[192:195]\n"
"ds_read_b64_tr_b16 a[218:219], v14 offset:51456\n"
"ds_read_b64_tr_b16 a[220:221], v15 offset:49408\n"
"v_mfma_f32_16x16x32_bf16 v[196:199], a[196:199], v[92:95], v[196:199]\n"
"buffer_load_dwordx4 v[188:191], v126, s[16:19], s23 offen offset:0 sc1\n"
"ds_read_b64_tr_b16 a[222:223], v15 offset:51456\n"
"v_mfma_f32_16x16x32_bf16 v[200:203], a[200:203], v[92:95], v[200:203]\n"
"v_mfma_f32_16x16x32_bf16 v[204:207], a[204:207], v[92:95], v[204:207]\n"
"s_waitcnt vmcnt(17)\n"
"v_mfma_f32_16x16x32_bf16 v[192:195], a[0:3], v[96:99], v[192:195]\n"
"v_mul_u32_u24 v18, 272, v2\n"
"v_lshl_add_u32 v18, v1, 4, v18\n"
"v_mfma_f32_16x16x32_bf16 v[196:199], a[4:7], v[96:99], v[196:199]\n"
"buffer_load_dwordx4 v[64:67], v120, s[16:19], s23 offen offset:1024 sc1\n"
"s_add_u32 s30, s5, 1\n"
"v_mfma_f32_16x16x32_bf16 v[200:203], a[8:11], v[96:99], v[200:203]\n"
"s_and_b32 s30, s30, 3\n"
"s_mul_i32 s30, s30, 17408\n"
"v_mfma_f32_16x16x32_bf16 v[204:207], a[12:15], v[96:99], v[204:207]\n"
"s_add_u32 s30, s30, 16640\n"
"v_add_u32 v19, s30, v18\n"
"s_waitcnt vmcnt(17)\n"
"v_mfma_f32_16x16x32_bf16 v[192:195], a[16:19], v[100:103], v[192:195]\n"
"s_add_u32 s30, s5, 2\n"
"s_and_b32 s30, s30, 3\n"
"v_mfma_f32_16x16x32_bf16 v[196:199], a[20:23], v[100:103], v[196:199]\n"
"v_mov_b32 v4, s34\n"
"v_mov_b32 v5, s35\n"
"v_mov_b32 v6, s28\n"
"s_mov_b64 exec, 1\n"
"buffer_store_dwordx2 v[4:5], v6, s[16:19], 0 offen sc1\n"
"s_mov_b64 exec, -1\n"
"v_mfma_f32_16x16x32_bf16 v[200:203], a[24:27], v[100:103], v[200:203]\n"
"s_mul_i32 s30, s30, 17408\n"
"s_add_u32 s30, s30, 20992\n"
"v_mfma_f32_16x16x32_bf16 v[204:207], a[28:31], v[100:103], v[204:207]\n"
"s_waitcnt lgkmcnt(0)\n"
"v_add_u32 v20, s30, v18\n"
"s_waitcnt vmcnt(17)\n"
"v_mfma_f32_16x16x32_bf16 v[192:195], a[32:35], v[104:107], v[192:195]\n"
"s_add_u32 s30, s5, 3\n"
"s_and_b32 s30, s30, 3\n"
"v_mfma_f32_16x16x32_bf16 v[196:199], a[36:39], v[104:107], v[196:199]\n"
"buffer_load_dwordx4 v[128:131], v121, s[16:19], s23 offen offset:1024 sc1\n"
"s_mul_i32 s30, s30, 17408\n"
"v_mfma_f32_16x16x32_bf16 v[200:203], a[40:43], v[104:107], v[200:203]\n"
"s_add_u32 s30, s30, 25344\n"
"v_add_u32 v21, s30, v18\n"
"v_mfma_f32_16x16x32_bf16 v[204:207], a[44:47], v[104:107], v[204:207]\n"
"s_mul_i32 s30, s5, 17408\n"
"s_add_u32 s30, s30, 16640\n"
"s_waitcnt vmcnt(13)\n"
"v_mfma_f32_16x16x32_bf16 v[192:195], a[48:51], v[108:111], v[192:195]\n"
"v_add_u32 v18, s30, v18\n"
"s_lshl_b32 s30, s5, 5\n"
"v_mfma_f32_16x16x32_bf16 v[196:199], a[52:55], v[108:111], v[196:199]\n"
"buffer_load_dwordx4 v[132:135], v122, s[16:19], s23 offen offset:1024 sc1\n"
"v_xor_b32 v29, s30, v29\n"
"v_mfma_f32_16x16x32_bf16 v[200:203], a[56:59], v[108:111], v[200:203]\n"
"v_add_u32 v29, 86272, v29\n"
"v_bfe_u32 v28, v0, 2, 3\n"
"v_mfma_f32_16x16x32_bf16 v[204:207], a[60:63], v[108:111], v[204:207]\n"
"v_lshlrev_b32 v28, 6, v28\n"
"v_and_b32 v3, 3, v0\n"
"s_waitcnt vmcnt(13)\n"
"v_mfma_f32_16x16x32_bf16 v[192:195], a[64:67], v[112:115], v[192:195]\n"
"s_waitcnt lgkmcnt(0)\n"
"s_barrier\n"
"v_mfma_f32_16x16x32_bf16 v[196:199], a[68:71], v[112:115], v[196:199]\n"
"buffer_load_dwordx4 v[136:139], v123, s[16:19], s23 offen offset:1024 sc1\n"
"s_waitcnt vmcnt(62)\n"
"v_mfma_f32_16x16x32_bf16 v[200:203], a[72:75], v[112:115], v[200:203]\n"
"ds_write_b128 v36, v[40:43]\n"
"v_lshl_add_u32 v28, v3, 3, v28\n"
"v_mfma_f32_16x16x32_bf16 v[204:207], a[76:79], v[112:115], v[204:207]\n"
"s_lshl_b32 s28, s49, 11\n"
"s_lshl_b32 s29, s7, 9\n"
"s_waitcnt vmcnt(13)\n"
"v_mfma_f32_16x16x32_bf16 v[192:195], a[80:83], v[116:119], v[192:195]\n"
"s_add_u32 s28, s28, s29\n"
"s_add_u32 s28, s28, 0x8000\n"
"v_mfma_f32_16x16x32_bf16 v[196:199], a[84:87], v[116:119], v[196:199]\n"
"buffer_load_dwordx4 v[140:143], v124, s[16:19], s23 offen offset:1024 sc1\n"
"ds_write_b128 v36, v[44:47] offset:64\n"
"v_mfma_f32_16x16x32_bf16 v[200:203], a[88:91], v[116:119], v[200:203]\n"
"v_add_u32 v28, s28, v28\n"
"v_mfma_f32_16x16x32_bf16 v[204:207], a[92:95], v[116:119], v[204:207]\n"
"s_waitcnt vmcnt(13)\n"
"v_mfma_f32_16x16x32_bf16 v[192:195], a[208:211], v[160:163], v[192:195]\n"
"v_mfma_f32_16x16x32_bf16 v[196:199], a[212:215], v[160:163], v[196:199]\n"
"buffer_load_dwordx4 v[68:71], v125, s[16:19], s23 offen offset:1024 sc1\n"
"v_mfma_f32_16x16x32_bf16 v[200:203], a[216:219], v[160:163], v[200:203]\n"
"v_mfma_f32_16x16x32_bf16 v[204:207], a[220:223], v[160:163], v[204:207]\n"
"s_waitcnt vmcnt(13)\n"
"v_mfma_f32_16x16x32_bf16 v[208:211], a[96:99], v[164:167], 0\n"
"v_mfma_f32_16x16x32_bf16 v[212:215], a[100:103], v[164:167], 0\n"
"buffer_load_dwordx4 v[72:75], v126, s[16:19], s23 offen offset:1024 sc1\n"
"s_waitcnt lgkmcnt(0)\n"
"v_mfma_f32_16x16x32_bf16 v[216:219], a[104:107], v[164:167], 0\n"
"ds_write_b128 v19, v[192:195] offset:0\n"
"ds_read_b64 v[40:41], v37 offset:0\n"
"v_mfma_f32_16x16x32_bf16 v[220:223], a[108:111], v[164:167], 0\n"
"ds_read_b64 v[42:43], v37 offset:32\n"
"ds_read_b64 v[44:45], v37 offset:64\n"
"s_waitcnt vmcnt(13)\n"
"v_mfma_f32_16x16x32_bf16 v[208:211], a[112:115], v[168:171], v[208:211]\n"
"ds_read_b64 v[46:47], v37 offset:96\n"
"v_mfma_f32_16x16x32_bf16 v[212:215], a[116:119], v[168:171], v[212:215]\n"
"ds_write_b128 v19, v[196:199] offset:64\n"
"buffer_load_dwordx4 v[76:79], v120, s[16:19], s25 offen offset:0 sc1\n"
"v_mfma_f32_16x16x32_bf16 v[216:219], a[120:123], v[168:171], v[216:219]\n"
"v_mfma_f32_16x16x32_bf16 v[220:223], a[124:127], v[168:171], v[220:223]\n"
"s_waitcnt vmcnt(13)\n"
"v_mfma_f32_16x16x32_bf16 v[208:211], a[128:131], v[172:175], v[208:211]\n"
"ds_write_b128 v19, v[200:203] offset:128\n"
"v_mfma_f32_16x16x32_bf16 v[212:215], a[132:135], v[172:175], v[212:215]\n"
"buffer_load_dwordx4 v[80:83], v121, s[16:19], s25 offen offset:0 sc1\n"
"v_mfma_f32_16x16x32_bf16 v[216:219], a[136:139], v[172:175], v[216:219]\n"
"v_mfma_f32_16x16x32_bf16 v[220:223], a[140:143], v[172:175], v[220:223]\n"
"ds_write_b128 v19, v[204:207] offset:192\n"
"s_waitcnt vmcnt(13)\n"
"v_mfma_f32_16x16x32_bf16 v[208:211], a[144:147], v[176:179], v[208:211]\n"
"s_waitcnt lgkmcnt(3)\n"
"v_lshlrev_b32 v224, 16, v40\n"
"v_mfma_f32_16x16x32_bf16 v[212:215], a[148:151], v[176:179], v[212:215]\n"
"buffer_load_dwordx4 v[84:87], v122, s[16:19], s25 offen offset:0 sc1\n"
"v_and_b32 v225, 0xffff0000, v40\n"
"v_mfma_f32_16x16x32_bf16 v[216:219], a[152:155], v[176:179], v[216:219]\n"
"v_lshlrev_b32 v226, 16, v41\n"
"v_and_b32 v227, 0xffff0000, v41\n"
"v_mfma_f32_16x16x32_bf16 v[220:223], a[156:159], v[176:179], v[220:223]\n"
"v_lshlrev_b32 v228, 16, v42\n"
"v_and_b32 v229, 0xffff0000, v42\n"
"s_waitcnt vmcnt(13)\n"
"v_mfma_f32_16x16x32_bf16 v[208:211], a[160:163], v[180:183], v[208:211]\n"
"v_lshlrev_b32 v230, 16, v43\n"
"v_and_b32 v231, 0xffff0000, v43\n"
"v_mfma_f32_16x16x32_bf16 v[212:215], a[164:167], v[180:183], v[212:215]\n"
"buffer_load_dwordx4 v[88:91], v123, s[16:19], s25 offen offset:0 sc1\n"
"v_lshlrev_b32 v232, 16, v44\n"
"v_mfma_f32_16x16x32_bf16 v[216:219], a[168:171], v[180:183], v[216:219]\n"
"v_and_b32 v233, 0xffff0000, v44\n"
"v_lshlrev_b32 v234, 16, v45\n"
"v_mfma_f32_16x16x32_bf16 v[220:223], a[172:175], v[180:183], v[220:223]\n"
"v_and_b32 v235, 0xffff0000, v45\n"
"v_lshlrev_b32 v236, 16, v46\n"
"s_waitcnt vmcnt(13)\n"
"v_mfma_f32_16x16x32_bf16 v[208:211], a[176:179], v[184:187], v[208:211]\n"
"v_and_b32 v237, 0xffff0000, v46\n"
"v_lshlrev_b32 v238, 16, v47\n"
"v_mfma_f32_16x16x32_bf16 v[212:215], a[180:183], v[184:187], v[212:215]\n"
"buffer_load_dwordx4 v[92:95], v124, s[16:19], s25 offen offset:0 sc1\n"
"v_and_b32 v239, 0xffff0000, v47\n"
"v_mfma_f32_16x16x32_bf16 v[216:219], a[184:187], v[184:187], v[216:219]\n"
"s_waitcnt lgkmcnt(0)\n"
"s_barrier\n"
"v_mfma_f32_16x16x32_bf16 v[220:223], a[188:191], v[184:187], v[220:223]\n"
"v_sub_f32 v240, v224, v48\n"
"v_sub_f32 v241, v225, v48\n"
"s_waitcnt vmcnt(13)\n"
"v_mfma_f32_16x16x32_bf16 v[208:211], a[192:195], v[188:191], v[208:211]\n"
"v_sub_f32 v242, v226, v48\n"
"v_sub_f32 v243, v227, v48\n"
"v_mfma_f32_16x16x32_bf16 v[212:215], a[196:199], v[188:191], v[212:215]\n"
"buffer_load_dwordx4 v[96:99], v125, s[16:19], s25 offen offset:0 sc1\n"
"v_sub_f32 v244, v228, v48\n"
"v_mfma_f32_16x16x32_bf16 v[216:219], a[200:203], v[188:191], v[216:219]\n"
"v_sub_f32 v245, v229, v48\n"
"v_sub_f32 v246, v230, v48\n"
"v_mfma_f32_16x16x32_bf16 v[220:223], a[204:207], v[188:191], v[220:223]\n"
"v_sub_f32 v247, v231, v48\n"
"v_sub_f32 v248, v232, v48\n"
"s_waitcnt vmcnt(13)\n"
"v_mfma_f32_16x16x32_bf16 v[208:211], a[0:3], v[64:67], v[208:211]\n"
"v_sub_f32 v249, v233, v48\n"
"v_sub_f32 v250, v234, v48\n"
"v_mfma_f32_16x16x32_bf16 v[212:215], a[4:7], v[64:67], v[212:215]\n"
"buffer_load_dwordx4 v[100:103], v126, s[16:19], s25 offen offset:0 sc1\n"
"v_sub_f32 v251, v235, v48\n"
"v_mfma_f32_16x16x32_bf16 v[216:219], a[8:11], v[64:67], v[216:219]\n"
"v_sub_f32 v252, v236, v48\n"
"v_sub_f32 v253, v237, v48\n"
"v_mfma_f32_16x16x32_bf16 v[220:223], a[12:15], v[64:67], v[220:223]\n"
"v_sub_f32 v254, v238, v48\n"
"v_sub_f32 v255, v239, v48\n"
"s_waitcnt vmcnt(12)\n"
"v_mfma_f32_16x16x32_bf16 v[208:211], a[16:19], v[128:131], v[208:211]\n"
"v_mul_f32 v240, v240, v49\n"
"v_mul_f32 v241, v241, v49\n"
"v_mfma_f32_16x16x32_bf16 v[212:215], a[20:23], v[128:131], v[212:215]\n"
"buffer_load_dwordx4 v[104:107], v120, s[16:19], s25 offen offset:1024 sc1\n"
"v_mul_f32 v242, v242, v49\n"
"v_mfma_f32_16x16x32_bf16 v[216:219], a[24:27], v[128:131], v[216:219]\n"
"v_mul_f32 v243, v243, v49\n"
"v_mul_f32 v244, v244, v49\n"
"v_mfma_f32_16x16x32_bf16 v[220:223], a[28:31], v[128:131], v[220:223]\n"
"v_mul_f32 v245, v245, v49\n"
"v_mul_f32 v246, v246, v49\n"
"s_waitcnt vmcnt(12)\n"
"v_mfma_f32_16x16x32_bf16 v[208:211], a[32:35], v[132:135], v[208:211]\n"
"v_mul_f32 v247, v247, v49\n"
"v_mul_f32 v248, v248, v49\n"
"v_mfma_f32_16x16x32_bf16 v[212:215], a[36:39], v[132:135], v[212:215]\n"
"buffer_load_dwordx4 v[108:111], v121, s[16:19], s25 offen offset:1024 sc1\n"
"v_mul_f32 v249, v249, v49\n"
"v_mfma_f32_16x16x32_bf16 v[216:219], a[40:43], v[132:135], v[216:219]\n"
"v_mul_f32 v250, v250, v49\n"
"v_mul_f32 v251, v251, v49\n"
"v_mfma_f32_16x16x32_bf16 v[220:223], a[44:47], v[132:135], v[220:223]\n"
"v_mul_f32 v252, v252, v49\n"
"v_mul_f32 v253, v253, v49\n"
"s_waitcnt vmcnt(12)\n"
"v_mfma_f32_16x16x32_bf16 v[208:211], a[48:51], v[136:139], v[208:211]\n"
"v_mul_f32 v254, v254, v49\n"
"v_mul_f32 v255, v255, v49\n"
"v_mfma_f32_16x16x32_bf16 v[212:215], a[52:55], v[136:139], v[212:215]\n"
"buffer_load_dwordx4 v[112:115], v122, s[16:19], s25 offen offset:1024 sc1\n"
"ds_read_b128 v[144:147], v18 offset:0\n"
"v_mfma_f32_16x16x32_bf16 v[216:219], a[56:59], v[136:139], v[216:219]\n"
"ds_read_b128 v[148:151], v18 offset:64\n"
"ds_read_b128 v[152:155], v18 offset:128\n"
"v_mfma_f32_16x16x32_bf16 v[220:223], a[60:63], v[136:139], v[220:223]\n"
"ds_read_b128 v[156:159], v18 offset:192\n"
"s_waitcnt vmcnt(12)\n"
"v_mfma_f32_16x16x32_bf16 v[208:211], a[64:67], v[140:143], v[208:211]\n"
"v_mfma_f32_16x16x32_bf16 v[212:215], a[68:71], v[140:143], v[212:215]\n"
"buffer_load_dwordx4 v[116:119], v123, s[16:19], s25 offen offset:1024 sc1\n"
"v_mfma_f32_16x16x32_bf16 v[216:219], a[72:75], v[140:143], v[216:219]\n"
"v_mfma_f32_16x16x32_bf16 v[220:223], a[76:79], v[140:143], v[220:223]\n"
"s_waitcnt vmcnt(12)\n"
"v_mfma_f32_16x16x32_bf16 v[208:211], a[80:83], v[68:71], v[208:211]\n"
"v_mfma_f32_16x16x32_bf16 v[212:215], a[84:87], v[68:71], v[212:215]\n"
"buffer_load_dwordx4 v[160:163], v124, s[16:19], s25 offen offset:1024 sc1\n"
"v_mfma_f32_16x16x32_bf16 v[216:219], a[88:91], v[68:71], v[216:219]\n"
"v_mfma_f32_16x16x32_bf16 v[220:223], a[92:95], v[68:71], v[220:223]\n"
"s_waitcnt lgkmcnt(0)\n"
"v_add_f32 v224, v224, v144\n"
"s_waitcnt vmcnt(12)\n"
"v_mfma_f32_16x16x32_bf16 v[208:211], a[208:211], v[72:75], v[208:211]\n"
"v_add_f32 v225, v225, v145\n"
"v_add_f32 v226, v226, v146\n"
"v_mfma_f32_16x16x32_bf16 v[212:215], a[212:215], v[72:75], v[212:215]\n"
"buffer_load_dwordx4 v[164:167], v125, s[16:19], s25 offen offset:1024 sc1\n"
"v_add_f32 v227, v227, v147\n"
"v_mfma_f32_16x16x32_bf16 v[216:219], a[216:219], v[72:75], v[216:219]\n"
"v_add_f32 v228, v228, v148\n"
"v_add_f32 v229, v229, v149\n"
"v_mfma_f32_16x16x32_bf16 v[220:223], a[220:223], v[72:75], v[220:223]\n"
"v_add_f32 v230, v230, v150\n"
"v_add_f32 v231, v231, v151\n"
"s_waitcnt vmcnt(12)\n"
"v_mfma_f32_16x16x32_bf16 v[192:195], a[96:99], v[76:79], 0\n"
"v_add_f32 v232, v232, v152\n"
"v_add_f32 v233, v233, v153\n"
"v_mfma_f32_16x16x32_bf16 v[196:199], a[100:103], v[76:79], 0\n"
"buffer_load_dwordx4 v[168:171], v126, s[16:19], s25 offen offset:1024 sc1\n"
"v_add_f32 v234, v234, v154\n"
"v_mfma_f32_16x16x32_bf16 v[200:203], a[104:107], v[76:79], 0\n"
"ds_write_b128 v20, v[208:211] offset:0\n"
"v_add_f32 v235, v235, v155\n"
"v_mfma_f32_16x16x32_bf16 v[204:207], a[108:111], v[76:79], 0\n"
"v_add_f32 v236, v236, v156\n"
"v_add_f32 v237, v237, v157\n"
"s_waitcnt vmcnt(12)\n"
"v_mfma_f32_16x16x32_bf16 v[192:195], a[112:115], v[80:83], v[192:195]\n"
"v_add_f32 v238, v238, v158\n"
"v_add_f32 v239, v239, v159\n"
"v_mfma_f32_16x16x32_bf16 v[196:199], a[116:119], v[80:83], v[196:199]\n"
"ds_write_b128 v20, v[212:215] offset:64\n"
"buffer_load_dwordx4 v[172:175], v120, s[16:19], s27 offen offset:0 sc1\n"
"v_mfma_f32_16x16x32_bf16 v[200:203], a[120:123], v[80:83], v[200:203]\n"
"v_mfma_f32_16x16x32_bf16 v[204:207], a[124:127], v[80:83], v[204:207]\n"
"s_waitcnt vmcnt(12)\n"
"v_mfma_f32_16x16x32_bf16 v[192:195], a[128:131], v[84:87], v[192:195]\n"
"ds_write_b128 v20, v[216:219] offset:128\n"
"v_mfma_f32_16x16x32_bf16 v[196:199], a[132:135], v[84:87], v[196:199]\n"
"buffer_load_dwordx2 v[26:27], v28, s[16:19], 0 offen sc1\n"
"v_mfma_f32_16x16x32_bf16 v[200:203], a[136:139], v[84:87], v[200:203]\n"
"v_mfma_f32_16x16x32_bf16 v[204:207], a[140:143], v[84:87], v[204:207]\n"
"ds_write_b128 v20, v[220:223] offset:192\n"
"s_waitcnt vmcnt(12)\n"
"v_mfma_f32_16x16x32_bf16 v[192:195], a[144:147], v[88:91], v[192:195]\n"
"v_mfma_f32_16x16x32_bf16 v[196:199], a[148:151], v[88:91], v[196:199]\n"
"v_mfma_f32_16x16x32_bf16 v[200:203], a[152:155], v[88:91], v[200:203]\n"
"v_mfma_f32_16x16x32_bf16 v[204:207], a[156:159], v[88:91], v[204:207]\n"
"s_waitcnt vmcnt(11)\n"
"v_mfma_f32_16x16x32_bf16 v[192:195], a[160:163], v[92:95], v[192:195]\n"
"v_mfma_f32_16x16x32_bf16 v[196:199], a[164:167], v[92:95], v[196:199]\n"
"buffer_load_dwordx4 v[176:179], v121, s[16:19], s27 offen offset:0 sc1\n"
"v_mfma_f32_16x16x32_bf16 v[200:203], a[168:171], v[92:95], v[200:203]\n"
"v_mfma_f32_16x16x32_bf16 v[204:207], a[172:175], v[92:95], v[204:207]\n"
"s_waitcnt vmcnt(11)\n"
"v_mfma_f32_16x16x32_bf16 v[192:195], a[176:179], v[96:99], v[192:195]\n"
"v_mfma_f32_16x16x32_bf16 v[196:199], a[180:183], v[96:99], v[196:199]\n"
"buffer_load_dwordx4 v[180:183], v122, s[16:19], s27 offen offset:0 sc1\n"
"s_waitcnt vmcnt(2)\n"
"v_cmp_ne_u64 vcc, s[34:35], v[26:27]\n"
"s_nop 4\n"
"s_cbranch_vccnz .L_slow_y%=\n"
".L_back_y%=:\n"
"v_mfma_f32_16x16x32_bf16 v[200:203], a[184:187], v[96:99], v[200:203]\n"
"s_waitcnt lgkmcnt(0)\n"
"s_barrier\n"
"v_mfma_f32_16x16x32_bf16 v[204:207], a[188:191], v[96:99], v[204:207]\n"
"s_lshl_b32 s28, s49, 6\n"
"s_lshl_b32 s29, s7, 4\n"
"s_waitcnt vmcnt(11)\n"
"v_mfma_f32_16x16x32_bf16 v[192:195], a[192:195], v[100:103], v[192:195]\n"
"s_add_u32 s28, s28, s29\n"
"v_lshrrev_b32 v4, 2, v0\n"
"v_mfma_f32_16x16x32_bf16 v[196:199], a[196:199], v[100:103], v[196:199]\n"
"buffer_load_dwordx4 v[184:187], v123, s[16:19], s27 offen offset:0 sc1\n"
"v_add_u32 v5, s28, v4\n"
"v_mfma_f32_16x16x32_bf16 v[200:203], a[200:203], v[100:103], v[200:203]\n"
"v_mul_u32_u24 v5, 4224, v5\n"
"v_and_b32 v6, 3, v0\n"
"v_mfma_f32_16x16x32_bf16 v[204:207], a[204:207], v[100:103], v[204:207]\n"
"v_lshl_add_u32 v5, v6, 4, v5\n"
"s_lshl_b32 s28, s48, 7\n"
"s_waitcnt vmcnt(11)\n"
"v_mfma_f32_16x16x32_bf16 v[192:195], a[0:3], v[104:107], v[192:195]\n"
"s_add_u32 s28, s28, 0x340000\n"
"ds_read_b128 v[144:147], v18 offset:4352\n"
"v_mfma_f32_16x16x32_bf16 v[196:199], a[4:7], v[104:107], v[196:199]\n"
"buffer_load_dwordx4 v[8:11], v5, s[16:19], s28 offen offset:0 sc1\n"
"ds_read_b128 v[148:151], v18 offset:4416\n"
"v_mfma_f32_16x16x32_bf16 v[200:203], a[8:11], v[104:107], v[200:203]\n"
"ds_read_b128 v[152:155], v18 offset:4480\n"
"ds_read_b128 v[156:159], v18 offset:4544\n"
"v_mfma_f32_16x16x32_bf16 v[204:207], a[12:15], v[104:107], v[204:207]\n"
"s_waitcnt vmcnt(11)\n"
"v_mfma_f32_16x16x32_bf16 v[192:195], a[16:19], v[108:111], v[192:195]\n"
"v_mfma_f32_16x16x32_bf16 v[196:199], a[20:23], v[108:111], v[196:199]\n"
"buffer_load_dwordx4 v[12:15], v5, s[16:19], s28 offen offset:64 sc1\n"
"v_bfe_u32 v53, v0, 3, 2\n"
"v_mfma_f32_16x16x32_bf16 v[200:203], a[24:27], v[108:111], v[200:203]\n"
"v_lshlrev_b32 v53, 1, v53\n"
"v_xor_b32 v53, v53, v6\n"
"v_mfma_f32_16x16x32_bf16 v[204:207], a[28:31], v[108:111], v[204:207]\n"
"v_lshlrev_b32 v53, 4, v53\n"
"v_lshl_add_u32 v53, v4, 7, v53\n"
"s_waitcnt vmcnt(11)\n"
"v_mfma_f32_16x16x32_bf16 v[192:195], a[32:35], v[112:115], v[192:195]\n"
"s_lshl_b32 s28, s7, 11\n"
"s_add_u32 s28, s28, 86272\n"
"v_mfma_f32_16x16x32_bf16 v[196:199], a[36:39], v[112:115], v[196:199]\n"
"buffer_load_dwordx4 v[188:191], v124, s[16:19], s27 offen offset:0 sc1\n"
"v_add_u32 v53, s28, v53\n"
"v_mfma_f32_16x16x32_bf16 v[200:203], a[40:43], v[112:115], v[200:203]\n"
"s_waitcnt lgkmcnt(0)\n"
"v_add_f32 v224, v224, v144\n"
"v_mfma_f32_16x16x32_bf16 v[204:207], a[44:47], v[112:115], v[204:207]\n"
"v_add_f32 v225, v225, v145\n"
"v_add_f32 v226, v226, v146\n"
"s_waitcnt vmcnt(11)\n"
"v_mfma_f32_16x16x32_bf16 v[192:195], a[48:51], v[116:119], v[192:195]\n"
"v_add_f32 v227, v227, v147\n"
"v_add_f32 v228, v228, v148\n"
"v_mfma_f32_16x16x32_bf16 v[196:199], a[52:55], v[116:119], v[196:199]\n"
"buffer_load_dwordx4 v[64:67], v125, s[16:19], s27 offen offset:0 sc1\n"
"v_add_f32 v229, v229, v149\n"
"v_mfma_f32_16x16x32_bf16 v[200:203], a[56:59], v[116:119], v[200:203]\n"
"v_add_f32 v230, v230, v150\n"
"v_add_f32 v231, v231, v151\n"
"v_mfma_f32_16x16x32_bf16 v[204:207], a[60:63], v[116:119], v[204:207]\n"
"v_add_f32 v232, v232, v152\n"
"v_add_f32 v233, v233, v153\n"
"s_waitcnt vmcnt(11)\n"
"v_mfma_f32_16x16x32_bf16 v[192:195], a[64:67], v[160:163], v[192:195]\n"
"v_add_f32 v234, v234, v154\n"
"v_add_f32 v235, v235, v155\n"
"v_mfma_f32_16x16x32_bf16 v[196:199], a[68:71], v[160:163], v[196:199]\n"
"buffer_load_dwordx4 v[128:131], v126, s[16:19], s27 offen offset:0 sc1\n"
"v_add_f32 v236, v236, v156\n"
"v_mfma_f32_16x16x32_bf16 v[200:203], a[72:75], v[160:163], v[200:203]\n"
"v_add_f32 v237, v237, v157\n"
"v_add_f32 v238, v238, v158\n"
"v_mfma_f32_16x16x32_bf16 v[204:207], a[76:79], v[160:163], v[204:207]\n"
"v_add_f32 v239, v239, v159\n"
"s_waitcnt vmcnt(11)\n"
"v_mfma_f32_16x16x32_bf16 v[192:195], a[80:83], v[164:167], v[192:195]\n"
"v_mfma_f32_16x16x32_bf16 v[196:199], a[84:87], v[164:167], v[196:199]\n"
"buffer_load_dwordx4 v[132:135], v120, s[16:19], s27 offen offset:1024 sc1\n"
"v_mfma_f32_16x16x32_bf16 v[200:203], a[88:91], v[164:167], v[200:203]\n"
"v_mfma_f32_16x16x32_bf16 v[204:207], a[92:95], v[164:167], v[204:207]\n"
"s_waitcnt vmcnt(11)\n"
"v_mfma_f32_16x16x32_bf16 v[192:195], a[208:211], v[168:171], v[192:195]\n"
"v_mfma_f32_16x16x32_bf16 v[196:199], a[212:215], v[168:171], v[196:199]\n"
"buffer_load_dwordx4 v[136:139], v121, s[16:19], s27 offen offset:1024 sc1\n"
"v_mfma_f32_16x16x32_bf16 v[200:203], a[216:219], v[168:171], v[200:203]\n"
"v_mfma_f32_16x16x32_bf16 v[204:207], a[220:223], v[168:171], v[204:207]\n"
"s_waitcnt vmcnt(11)\n"
"v_mfma_f32_16x16x32_bf16 v[208:211], a[96:99], v[172:175], 0\n"
"v_mfma_f32_16x16x32_bf16 v[212:215], a[100:103], v[172:175], 0\n"
"buffer_load_dwordx4 v[140:143], v122, s[16:19], s27 offen offset:1024 sc1\n"
"v_mfma_f32_16x16x32_bf16 v[216:219], a[104:107], v[172:175], 0\n"
"ds_write_b128 v21, v[192:195] offset:0\n"
"v_mfma_f32_16x16x32_bf16 v[220:223], a[108:111], v[172:175], 0\n"
"s_waitcnt vmcnt(10)\n"
"v_mfma_f32_16x16x32_bf16 v[208:211], a[112:115], v[176:179], v[208:211]\n"
"v_mfma_f32_16x16x32_bf16 v[212:215], a[116:119], v[176:179], v[212:215]\n"
"ds_write_b128 v21, v[196:199] offset:64\n"
"buffer_load_dwordx4 v[68:71], v123, s[16:19], s27 offen offset:1024 sc1\n"
"v_mfma_f32_16x16x32_bf16 v[216:219], a[120:123], v[176:179], v[216:219]\n"
"v_mfma_f32_16x16x32_bf16 v[220:223], a[124:127], v[176:179], v[220:223]\n"
"s_waitcnt vmcnt(10)\n"
"v_mfma_f32_16x16x32_bf16 v[208:211], a[128:131], v[180:183], v[208:211]\n"
"ds_write_b128 v21, v[200:203] offset:128\n"
"v_mfma_f32_16x16x32_bf16 v[212:215], a[132:135], v[180:183], v[212:215]\n"
"buffer_load_dwordx4 v[72:75], v124, s[16:19], s27 offen offset:1024 sc1\n"
"v_mfma_f32_16x16x32_bf16 v[216:219], a[136:139], v[180:183], v[216:219]\n"
"v_mfma_f32_16x16x32_bf16 v[220:223], a[140:143], v[180:183], v[220:223]\n"
"ds_write_b128 v21, v[204:207] offset:192\n"
"s_waitcnt vmcnt(10)\n"
"v_mfma_f32_16x16x32_bf16 v[208:211], a[144:147], v[184:187], v[208:211]\n"
"v_mfma_f32_16x16x32_bf16 v[212:215], a[148:151], v[184:187], v[212:215]\n"
"buffer_load_dwordx4 v[76:79], v125, s[16:19], s27 offen offset:1024 sc1\n"
"v_mfma_f32_16x16x32_bf16 v[216:219], a[152:155], v[184:187], v[216:219]\n"
"s_waitcnt vmcnt(9)\n"
"ds_write_b128 v53, v[8:11]\n"
"v_mfma_f32_16x16x32_bf16 v[220:223], a[156:159], v[184:187], v[220:223]\n"
"v_xor_b32 v53, 64, v53\n"
"ds_write_b128 v53, v[12:15]\n"
"s_waitcnt vmcnt(8)\n"
"v_mfma_f32_16x16x32_bf16 v[208:211], a[160:163], v[188:191], v[208:211]\n"
"v_mfma_f32_16x16x32_bf16 v[212:215], a[164:167], v[188:191], v[212:215]\n"
"buffer_load_dwordx4 v[80:83], v126, s[16:19], s27 offen offset:1024 sc1\n"
"v_mfma_f32_16x16x32_bf16 v[216:219], a[168:171], v[188:191], v[216:219]\n"
"v_mfma_f32_16x16x32_bf16 v[220:223], a[172:175], v[188:191], v[220:223]\n"
"s_waitcnt vmcnt(8)\n"
"v_mfma_f32_16x16x32_bf16 v[208:211], a[176:179], v[64:67], v[208:211]\n"
"v_mfma_f32_16x16x32_bf16 v[212:215], a[180:183], v[64:67], v[212:215]\n"
"v_mfma_f32_16x16x32_bf16 v[216:219], a[184:187], v[64:67], v[216:219]\n"
"s_waitcnt lgkmcnt(0)\n"
"s_barrier\n"
"v_mfma_f32_16x16x32_bf16 v[220:223], a[188:191], v[64:67], v[220:223]\n"
"ds_read_b64_tr_b16 v[24:25], v29 offset:0\n"
"ds_read_b64_tr_b16 v[26:27], v29 offset:2048\n"
"ds_read_b64_tr_b16 v[4:5], v29 offset:4096\n"
"ds_read_b64_tr_b16 v[6:7], v29 offset:6144\n"
"ds_read_b128 v[144:147], v18 offset:8704\n"
"ds_read_b128 v[148:151], v18 offset:8768\n"
"ds_read_b128 v[152:155], v18 offset:8832\n"
"ds_read_b128 v[156:159], v18 offset:8896\n"
"s_waitcnt vmcnt(7)\n"
"v_mfma_f32_16x16x32_bf16 v[208:211], a[192:195], v[128:131], v[208:211]\n"
"v_mfma_f32_16x16x32_bf16 v[212:215], a[196:199], v[128:131], v[212:215]\n"
"v_mfma_f32_16x16x32_bf16 v[216:219], a[200:203], v[128:131], v[216:219]\n"
"v_mfma_f32_16x16x32_bf16 v[220:223], a[204:207], v[128:131], v[220:223]\n"
"s_waitcnt lgkmcnt(4)\n"
"v_lshlrev_b32 v40, 16, v24\n"
"v_and_b32 v41, 0xffff0000, v24\n"
"v_fmac_f32 v224, v240, v40\n"
"v_fmac_f32 v225, v241, v41\n"
"v_lshlrev_b32 v40, 16, v25\n"
"v_and_b32 v41, 0xffff0000, v25\n"
"s_waitcnt vmcnt(6)\n"
"v_mfma_f32_16x16x32_bf16 v[208:211], a[0:3], v[132:135], v[208:211]\n"
"v_fmac_f32 v226, v242, v40\n"
"v_fmac_f32 v227, v243, v41\n"
"v_mfma_f32_16x16x32_bf16 v[212:215], a[4:7], v[132:135], v[212:215]\n"
"v_lshlrev_b32 v40, 16, v26\n"
"v_and_b32 v41, 0xffff0000, v26\n"
"v_mfma_f32_16x16x32_bf16 v[216:219], a[8:11], v[132:135], v[216:219]\n"
"v_fmac_f32 v228, v244, v40\n"
"v_fmac_f32 v229, v245, v41\n"
"v_mfma_f32_16x16x32_bf16 v[220:223], a[12:15], v[132:135], v[220:223]\n"
"v_lshlrev_b32 v40, 16, v27\n"
"v_and_b32 v41, 0xffff0000, v27\n"
"s_waitcnt vmcnt(5)\n"
"v_mfma_f32_16x16x32_bf16 v[208:211], a[16:19], v[136:139], v[208:211]\n"
"v_fmac_f32 v230, v246, v40\n"
"v_fmac_f32 v231, v247, v41\n"
"v_mfma_f32_16x16x32_bf16 v[212:215], a[20:23], v[136:139], v[212:215]\n"
"v_lshlrev_b32 v40, 16, v4\n"
"v_and_b32 v41, 0xffff0000, v4\n"
"v_mfma_f32_16x16x32_bf16 v[216:219], a[24:27], v[136:139], v[216:219]\n"
"v_fmac_f32 v232, v248, v40\n"
"v_fmac_f32 v233, v249, v41\n"
"v_mfma_f32_16x16x32_bf16 v[220:223], a[28:31], v[136:139], v[220:223]\n"
"v_lshlrev_b32 v40, 16, v5\n"
"v_and_b32 v41, 0xffff0000, v5\n"
"s_waitcnt vmcnt(4)\n"
"v_mfma_f32_16x16x32_bf16 v[208:211], a[32:35], v[140:143], v[208:211]\n"
"v_fmac_f32 v234, v250, v40\n"
"v_fmac_f32 v235, v251, v41\n"
"v_mfma_f32_16x16x32_bf16 v[212:215], a[36:39], v[140:143], v[212:215]\n"
"v_lshlrev_b32 v40, 16, v6\n"
"v_and_b32 v41, 0xffff0000, v6\n"
"v_mfma_f32_16x16x32_bf16 v[216:219], a[40:43], v[140:143], v[216:219]\n"
"v_fmac_f32 v236, v252, v40\n"
"v_fmac_f32 v237, v253, v41\n"
"v_mfma_f32_16x16x32_bf16 v[220:223], a[44:47], v[140:143], v[220:223]\n"
"v_lshlrev_b32 v40, 16, v7\n"
"v_and_b32 v41, 0xffff0000, v7\n"
"s_waitcnt vmcnt(3)\n"
"v_mfma_f32_16x16x32_bf16 v[208:211], a[48:51], v[68:71], v[208:211]\n"
"v_fmac_f32 v238, v254, v40\n"
"v_fmac_f32 v239, v255, v41\n"
"v_mfma_f32_16x16x32_bf16 v[212:215], a[52:55], v[68:71], v[212:215]\n"
"s_waitcnt lgkmcnt(0)\n"
"v_add_f32 v224, v224, v144\n"
"v_mfma_f32_16x16x32_bf16 v[216:219], a[56:59], v[68:71], v[216:219]\n"
"v_add_f32 v225, v225, v145\n"
"v_add_f32 v226, v226, v146\n"
"v_mfma_f32_16x16x32_bf16 v[220:223], a[60:63], v[68:71], v[220:223]\n"
"v_add_f32 v227, v227, v147\n"
"v_add_f32 v228, v228, v148\n"
"s_waitcnt vmcnt(2)\n"
"v_mfma_f32_16x16x32_bf16 v[208:211], a[64:67], v[72:75], v[208:211]\n"
"v_add_f32 v229, v229, v149\n"
"v_add_f32 v230, v230, v150\n"
"v_mfma_f32_16x16x32_bf16 v[212:215], a[68:71], v[72:75], v[212:215]\n"
"v_add_f32 v231, v231, v151\n"
"v_add_f32 v232, v232, v152\n"
"v_mfma_f32_16x16x32_bf16 v[216:219], a[72:75], v[72:75], v[216:219]\n"
"v_add_f32 v233, v233, v153\n"
"v_add_f32 v234, v234, v154\n"
"v_mfma_f32_16x16x32_bf16 v[220:223], a[76:79], v[72:75], v[220:223]\n"
"v_add_f32 v235, v235, v155\n"
"v_add_f32 v236, v236, v156\n"
"s_waitcnt vmcnt(1)\n"
"v_mfma_f32_16x16x32_bf16 v[208:211], a[80:83], v[76:79], v[208:211]\n"
"v_add_f32 v237, v237, v157\n"
"v_add_f32 v238, v238, v158\n"
"v_mfma_f32_16x16x32_bf16 v[212:215], a[84:87], v[76:79], v[212:215]\n"
"v_add_f32 v239, v239, v159\n"
"v_mfma_f32_16x16x32_bf16 v[216:219], a[88:91], v[76:79], v[216:219]\n"
"v_mfma_f32_16x16x32_bf16 v[220:223], a[92:95], v[76:79], v[220:223]\n"
"s_waitcnt vmcnt(0)\n"
"v_mfma_f32_16x16x32_bf16 v[208:211], a[208:211], v[80:83], v[208:211]\n"
"v_mfma_f32_16x16x32_bf16 v[212:215], a[212:215], v[80:83], v[212:215]\n"
"v_mfma_f32_16x16x32_bf16 v[216:219], a[216:219], v[80:83], v[216:219]\n"
"v_mfma_f32_16x16x32_bf16 v[220:223], a[220:223], v[80:83], v[220:223]\n"
"s_nop 4\n"
"v_pk_add_f32 v[224:225], v[224:225], v[208:209]\n"
"v_pk_add_f32 v[226:227], v[226:227], v[210:211]\n"
"v_cvt_pk_bf16_f32 v40, v224, v225\n"
"v_cvt_pk_bf16_f32 v41, v226, v227\n"
"ds_write_b64 v37, v[40:41] offset:0\n"
"v_pk_add_f32 v[228:229], v[228:229], v[212:213]\n"
"v_pk_add_f32 v[230:231], v[230:231], v[214:215]\n"
"v_cvt_pk_bf16_f32 v42, v228, v229\n"
"v_cvt_pk_bf16_f32 v43, v230, v231\n"
"ds_write_b64 v37, v[42:43] offset:32\n"
"v_pk_add_f32 v[232:233], v[232:233], v[216:217]\n"
"v_pk_add_f32 v[234:235], v[234:235], v[218:219]\n"
"v_cvt_pk_bf16_f32 v44, v232, v233\n"
"v_cvt_pk_bf16_f32 v45, v234, v235\n"
"ds_write_b64 v37, v[44:45] offset:64\n"
"v_pk_add_f32 v[236:237], v[236:237], v[220:221]\n"
"v_pk_add_f32 v[238:239], v[238:239], v[222:223]\n"
"v_cvt_pk_bf16_f32 v46, v236, v237\n"
"v_cvt_pk_bf16_f32 v47, v238, v239\n"
"ds_write_b64 v37, v[46:47] offset:96\n"
"s_waitcnt lgkmcnt(0)\n"
"ds_read_b128 v[8:11], v36 offset:0\n"
"ds_read_b128 v[12:15], v36 offset:64\n"
"s_waitcnt lgkmcnt(1)\n"
"buffer_store_dwordx4 v[8:11], v50, s[44:47], s59 offen offset:0\n"
"s_waitcnt lgkmcnt(0)\n"
"buffer_store_dwordx4 v[12:15], v50, s[44:47], s59 offen offset:64\n"
"s_branch .L_end%=\n"
".L_slow_st%=:\n"
"s_mov_b32 s31, 0x20000\n"
".L_spin_st%=:\n"
"s_sleep 1\n"
"buffer_load_dwordx2 v[26:27], v127, s[16:19], 0 offen sc1\n"
"s_waitcnt vmcnt(0)\n"
"v_cmp_ne_u64 vcc, s[34:35], v[26:27]\n"
"s_nop 4\n"
"s_cbranch_vccz .L_back_st%=\n"
"s_sub_u32 s31, s31, 1\n"
"s_cbranch_scc0 .L_spin_st%=\n"
"s_branch .L_back_st%=\n"
".L_slow_u%=:\n"
"s_mov_b32 s31, 0x20000\n"
".L_spin_u%=:\n"
"s_sleep 1\n"
"buffer_load_dwordx2 v[62:63], v126, s[16:19], 0 offen sc1\n"
"s_waitcnt vmcnt(0)\n"
"v_cmp_ne_u64 vcc, s[34:35], v[62:63]\n"
"s_nop 4\n"
"s_cbranch_vccz .L_back_u%=\n"
"s_sub_u32 s31, s31, 1\n"
"s_cbranch_scc0 .L_spin_u%=\n"
"s_branch .L_back_u%=\n"
".L_slow_go0%=:\n"
"s_mov_b32 s31, 0x20000\n"
".L_spin_go0%=:\n"
"s_sleep 1\n"
"buffer_load_dwordx2 v[4:5], v3, s[16:19], 0 offen sc1\n"
"s_waitcnt vmcnt(0)\n"
"v_cmp_ne_u64 vcc, s[34:35], v[4:5]\n"
"s_nop 4\n"
"s_cbranch_vccz .L_back_go0%=\n"
"s_sub_u32 s31, s31, 1\n"
"s_cbranch_scc0 .L_spin_go0%=\n"
"s_branch .L_back_go0%=\n"
".L_slow_go1%=:\n"
"s_mov_b32 s31, 0x20000\n"
".L_spin_go1%=:\n"
"s_sleep 1\n"
"buffer_load_dwordx2 v[4:5], v3, s[16:19], 0 offen sc1\n"
"s_waitcnt vmcnt(0)\n"
"v_cmp_ne_u64 vcc, s[34:35], v[4:5]\n"
"s_nop 4\n"
"s_cbranch_vccz .L_back_go1%=\n"
"s_sub_u32 s31, s31, 1\n"
"s_cbranch_scc0 .L_spin_go1%=\n"
"s_branch .L_back_go1%=\n"
".L_slow_go2%=:\n"
"s_mov_b32 s31, 0x20000\n"
".L_spin_go2%=:\n"
"s_sleep 1\n"
"buffer_load_dwordx2 v[4:5], v3, s[16:19], 0 offen sc1\n"
"s_waitcnt vmcnt(0)\n"
"v_cmp_ne_u64 vcc, s[34:35], v[4:5]\n"
"s_nop 4\n"
"s_cbranch_vccz .L_back_go2%=\n"
"s_sub_u32 s31, s31, 1\n"
"s_cbranch_scc0 .L_spin_go2%=\n"
"s_branch .L_back_go2%=\n"
".L_slow_go3%=:\n"
"s_mov_b32 s31, 0x20000\n"
".L_spin_go3%=:\n"
"s_sleep 1\n"
"buffer_load_dwordx2 v[4:5], v3, s[16:19], 0 offen sc1\n"
"s_waitcnt vmcnt(0)\n"
"v_cmp_ne_u64 vcc, s[34:35], v[4:5]\n"
"s_nop 4\n"
"s_cbranch_vccz .L_back_go3%=\n"
"s_sub_u32 s31, s31, 1\n"
"s_cbranch_scc0 .L_spin_go3%=\n"
"s_branch .L_back_go3%=\n"
".L_slow_u2%=:\n"
"s_mov_b32 s31, 0x20000\n"
".L_spin_u2%=:\n"
"s_sleep 1\n"
"buffer_load_dwordx2 v[62:63], v126, s[16:19], 0 offen sc1\n"
"s_waitcnt vmcnt(0)\n"
"v_cmp_ne_u64 vcc, s[34:35], v[62:63]\n"
"s_nop 4\n"
"s_cbranch_vccz .L_back_u2%=\n"
"s_sub_u32 s31, s31, 1\n"
"s_cbranch_scc0 .L_spin_u2%=\n"
"s_branch .L_back_u2%=\n"
".L_slow_h%=:\n"
"s_mov_b32 s31, 0x20000\n"
".L_spin_h%=:\n"
"s_sleep 1\n"
"buffer_load_dwordx2 v[62:63], v51, s[16:19], 0 offen sc1\n"
"s_waitcnt vmcnt(0)\n"
"v_cmp_ne_u64 vcc, s[34:35], v[62:63]\n"
"s_nop 4\n"
"s_cbranch_vccz .L_back_h%=\n"
"s_sub_u32 s31, s31, 1\n"
"s_cbranch_scc0 .L_spin_h%=\n"
"s_branch .L_back_h%=\n"
".L_slow_y%=:\n"
"s_mov_b32 s31, 0x20000\n"
".L_spin_y%=:\n"
"s_sleep 1\n"
"buffer_load_dwordx2 v[26:27], v28, s[16:19], 0 offen sc1\n"
"s_waitcnt vmcnt(0)\n"
"v_cmp_ne_u64 vcc, s[34:35], v[26:27]\n"
"s_nop 4\n"
"s_cbranch_vccz .L_back_y%=\n"
"s_sub_u32 s31, s31, 1\n"
"s_cbranch_scc0 .L_spin_y%=\n"
"s_branch .L_back_y%=\n"
".L_end%=:\n"
  : "+{v0}"(tid) : "s"(__builtin_amdgcn_kernarg_segment_ptr()), "s"(blockIdx.x) :
"v1","v2","v3","v4","v5","v6","v7","v8","v9","v10","v11","v12","v13","v14","v15","v16","v17","v18","v19","v20","v21","v22","v23","v24","v25","v26","v27","v28","v29","v30","v31","v32","v33","v34","v35","v36","v37","v38","v39","v40","v41","v42","v43","v44","v45","v46","v47","v48","v49","v50","v51","v52","v53","v54","v55","v56","v57","v58","v59","v60","v61","v62","v63","v64","v65","v66","v67","v68","v69","v70","v71","v72","v73","v74","v75","v76","v77","v78","v79","v80","v81","v82","v83","v84","v85","v86","v87","v88","v89","v90","v91","v92","v93","v94","v95","v96","v97","v98","v99","v100","v101","v102","v103","v104","v105","v106","v107","v108","v109","v110","v111","v112","v113","v114","v115","v116","v117","v118","v119","v120","v121","v122","v123","v124","v125","v126","v127","v128","v129","v130","v131","v132","v133","v134","v135","v136","v137","v138","v139","v140","v141","v142","v143","v144","v145","v146","v147","v148","v149","v150","v151","v152","v153","v154","v155","v156","v157","v158","v159","v160","v161","v162","v163","v164","v165","v166","v167","v168","v169","v170","v171","v172","v173","v174","v175","v176","v177","v178","v179","v180","v181","v182","v183","v184","v185","v186","v187","v188","v189","v190","v191","v192","v193","v194","v195","v196","v197","v198","v199","v200","v201","v202","v203","v204","v205","v206","v207","v208","v209","v210","v211","v212","v213","v214","v215","v216","v217","v218","v219","v220","v221","v222","v223","v224","v225","v226","v227","v228","v229","v230","v231","v232","v233","v234","v235","v236","v237","v238","v239","v240","v241","v242","v243","v244","v245","v246","v247","v248","v249","v250","v251","v252","v253","v254","v255",
"a0","a1","a2","a3","a4","a5","a6","a7","a8","a9","a10","a11","a12","a13","a14","a15","a16","a17","a18","a19","a20","a21","a22","a23","a24","a25","a26","a27","a28","a29","a30","a31","a32","a33","a34","a35","a36","a37","a38","a39","a40","a41","a42","a43","a44","a45","a46","a47","a48","a49","a50","a51","a52","a53","a54","a55","a56","a57","a58","a59","a60","a61","a62","a63","a64","a65","a66","a67","a68","a69","a70","a71","a72","a73","a74","a75","a76","a77","a78","a79","a80","a81","a82","a83","a84","a85","a86","a87","a88","a89","a90","a91","a92","a93","a94","a95","a96","a97","a98","a99","a100","a101","a102","a103","a104","a105","a106","a107","a108","a109","a110","a111","a112","a113","a114","a115","a116","a117","a118","a119","a120","a121","a122","a123","a124","a125","a126","a127","a128","a129","a130","a131","a132","a133","a134","a135","a136","a137","a138","a139","a140","a141","a142","a143","a144","a145","a146","a147","a148","a149","a150","a151","a152","a153","a154","a155","a156","a157","a158","a159","a160","a161","a162","a163","a164","a165","a166","a167","a168","a169","a170","a171","a172","a173","a174","a175","a176","a177","a178","a179","a180","a181","a182","a183","a184","a185","a186","a187","a188","a189","a190","a191","a192","a193","a194","a195","a196","a197","a198","a199","a200","a201","a202","a203","a204","a205","a206","a207","a208","a209","a210","a211","a212","a213","a214","a215","a216","a217","a218","a219","a220","a221","a222","a223","a224","a225","a226","a227","a228","a229","a230","a231","a232","a233","a234","a235","a236","a237","a238","a239","a240","a241","a242","a243","a244","a245","a246","a247","a248","a249","a250","a251","a252","a253","a254","a255",
"s4","s5","s6","s7","s8","s9","s10","s11","s12","s13","s14","s15","s16","s17","s18","s19","s20","s21","s22","s23","s24","s25","s26","s27","s28","s29","s30","s31","s34","s35","s36","s37","s38","s39","s40","s41","s42","s43","s44","s45","s46","s47","s48","s49","s50","s51","s52","s53","s54","s55","s56","s57","s58","s59","s60","s61","s62","s63","s64","s65","s66","s67","s68","s69","s70","s71","s72","s73","s74","s75","s76","s77","s78","s79","s80","s81","s82","s83","s84","s85","s86","s87","s88","s89","s90","s91","s92","s93","s94","s95","s96","s97","s98","s99","vcc","memory");
}
#define NSTAMP 0
extern "C" void kernel_launch(void* const* d_in, const int* in_sizes, int n_in, void* d_out, int out_size, void* d_ws, size_t ws_size, hipStream_t stream) {
  (void)hipFuncSetAttribute((const void*)h3_geglu_kernel, hipFuncAttributeMaxDynamicSharedMemorySize, 163840);
  h3_geglu_kernel<<<256, 256, 163840, stream>>>(d_in[0], d_in[1], d_in[2], d_in[3], d_in[4], d_in[5], d_in[7], d_in[8], d_in[9], d_out, d_ws);
}
```
